```python
import torch

def _softmax(x, axis=-1):
    return torch.softmax(x, dim=axis)
def _gelu(x):
    return torch.nn.functional.gelu(x, approximate="tanh")
def _silu(x):
    return torch.nn.functional.silu(x)
def _sigmoid(x):
    return torch.sigmoid(x)
def _layernorm(x, axis=-1, eps=1e-5):
    m = x.mean(dim=axis, keepdim=True)
    v = x.var(dim=axis, keepdim=True, unbiased=False)
    return (x - m) / torch.sqrt(v + eps)
def _scatter_add(tgt, idx, src, axis=0):
    return tgt.clone().index_add_(axis, idx, src)
def _segment_sum(src, seg, n):
    out = torch.zeros((n,) + src.shape[1:], dtype=src.dtype)
    return out.index_add_(0, seg, src)
def _pad(x, pw):
    flat = tuple(v for pair in reversed(pw) for v in pair)
    return torch.nn.functional.pad(x, flat)

kernel_name = 'gated_mix__linear_attn_block__geglu_ffn__prompt'

def setup_inputs():

    _w = (lambda a: (lambda t: t.to(torch.bfloat16) if t.is_floating_point() else t)(torch.as_tensor(a)))
    def _A_setup():
        B, S, D = 1, 2048, 576
        return {
            "x": _w((lambda *s: torch.randn(*s, dtype=torch.bfloat16) * 0.05)(B, S, D)),
            "wqkv": _w((lambda *s: torch.randn(*s, dtype=torch.bfloat16) * 0.8)(D, 3 * D)),
            "wo": _w((lambda *s: torch.randn(*s, dtype=torch.bfloat16) * 0.05)(D, D)),
        }
    _A_in = _A_setup()

    def _B_setup():
        B, S, D, F = 1, 2048, 576, 4608
        return {
            "x": _w((lambda *s: torch.randn(*s, dtype=torch.bfloat16) * 0.05)(B, S, D)),
            "w_gate": _w((lambda *s: torch.randn(*s, dtype=torch.bfloat16) * 0.05)(D, F)),
            "w_up": _w((lambda *s: torch.randn(*s, dtype=torch.bfloat16) * 0.05)(D, F)),
            "w_down": _w((lambda *s: torch.randn(*s, dtype=torch.bfloat16) * 0.05)(F, D)),
        }
    _B_in = _B_setup()

    return {
        "A_x": _A_in["x"],
        "A_wqkv": _A_in["wqkv"],
        "A_wo": _A_in["wo"],
        "B_x": _B_in["x"],
        "B_w_gate": _B_in["w_gate"],
        "B_w_up": _B_in["w_up"],
        "B_w_down": _B_in["w_down"],
        "gate_w": _w((lambda *s: torch.randn(*s, dtype=torch.bfloat16) * 0.05)(576) * 16.0),
    }

def reference(A_x, A_wqkv, A_wo, B_x, B_w_gate, B_w_up, B_w_down, gate_w):
    def _A_ref(x, wqkv, wo):
        B, S, D = x.shape
        H = 32; Dh = D // H
        qkv = (lambda x, *d: x.permute(*d))((x @ wqkv).reshape(B, S, 3, H, Dh), 2, 0, 3, 1, 4)
        q, k, v = qkv[0], qkv[1], qkv[2]
        phi = lambda t: torch.where(t > 0, t + 1, torch.exp(t)) + 1e-6
        pq, pk = phi(q), phi(k)
        kv = torch.einsum("bhsd,bhse->bhde", pk, v)
        z = torch.einsum("bhsd->bhd", pk)
        num = torch.einsum("bhqd,bhde->bhqe", pq, kv)
        den = torch.einsum("bhqd,bhd->bhq", pq, z)[..., None] + 1e-6
        o = num / den
        return (lambda x, *d: x.permute(*d))(o, 0, 2, 1, 3).reshape(B, S, D) @ wo

    def _B_ref(x, w_gate, w_up, w_down):
        return (_gelu(x @ w_gate) * (x @ w_up)) @ w_down

    x = A_x
    g = _sigmoid((x * gate_w).sum(-1, keepdims=True))
    a = _A_ref(x, A_wqkv, A_wo)
    b = _B_ref(x, B_w_gate, B_w_up, B_w_down)
    return g * a + (1 - g) * b
```

```cpp
#include <hip/hip_runtime.h>
#include <hip/hip_bf16.h>
#include <type_traits>

#define LAS __attribute__((address_space(3)))
typedef unsigned short u16;
typedef short s16x4 __attribute__((ext_vector_type(4)));
typedef short s16x8 __attribute__((ext_vector_type(8)));
typedef __bf16 bf16x8 __attribute__((ext_vector_type(8)));
typedef float f32x4 __attribute__((ext_vector_type(4)));
typedef unsigned u32x4 __attribute__((ext_vector_type(4)));
typedef unsigned u32x2 __attribute__((ext_vector_type(2)));

namespace {

constexpr int LD_X = 1152;
constexpr int LD_W = 9216;
constexpr int LD_QKV = 3456;

constexpr size_t WS_HMAIN = 0;
constexpr size_t WS_HLEFT = WS_HMAIN + 128ull * 128 * 1024;
constexpr size_t WS_PQM = WS_HLEFT + 128ull * 32 * 512;
constexpr size_t WS_PQR = WS_PQM + 128ull * 32 * 512;
constexpr size_t WS_G = WS_PQR + 32ull * 2048 * 4;
constexpr size_t WS_KVP = WS_G + 4ull * 2048 * 4;
constexpr size_t WS_SLAB = WS_KVP + 8ull * 32 * 4096;
constexpr size_t WS_FLAG = WS_SLAB + 64ull * 16 * 18432;
constexpr size_t WS_END = WS_FLAG + 256ull * 256;

struct Params {
  const char* x; const char* wqkv; const char* wo; const char* wg; const char* wu; const char* wd;
  const char* gw; char* out; char* ws;
};

template <int I> using ic = std::integral_constant<int, I>;
template <int B, int E, class Fn> __device__ __forceinline__ void sfor(Fn&& f) {
  if constexpr (B < E) { f(ic<B>{}); sfor<B + 1, E>(f); }
}

__device__ __forceinline__ void glds16(unsigned lds, unsigned voff, const char* sb) {
  asm volatile("s_mov_b32 m0, %0\n\ts_nop 0\n\tglobal_load_lds_dwordx4 %1, %2" ::"s"(lds), "v"(voff), "s"(sb));
}
__device__ __forceinline__ void glds4(unsigned lds, unsigned voff, const char* sb) {
  asm volatile("s_mov_b32 m0, %0\n\ts_nop 0\n\tglobal_load_lds_dword %1, %2" ::"s"(lds), "v"(voff), "s"(sb));
}
template <int N> __device__ __forceinline__ void wait_vm() {
  asm volatile("s_waitcnt vmcnt(%0)" ::"i"(N) : "memory");
}
__device__ __forceinline__ void bar() {
  asm volatile("" ::: "memory");
  __builtin_amdgcn_s_barrier();
  asm volatile("" ::: "memory");
}
__device__ __forceinline__ void wait_lds_bar() {
  asm volatile("s_waitcnt lgkmcnt(0)" ::: "memory");
  __builtin_amdgcn_s_barrier();
  asm volatile("" ::: "memory");
}
__device__ __forceinline__ bf16x8 tr_frag(unsigned a0, unsigned a1) {
  s16x4 lo = __builtin_amdgcn_ds_read_tr16_b64_v4i16((LAS s16x4*)(size_t)a0);
  s16x4 hi = __builtin_amdgcn_ds_read_tr16_b64_v4i16((LAS s16x4*)(size_t)a1);
  s16x8 f = __builtin_shufflevector(lo, hi, 0, 1, 2, 3, 4, 5, 6, 7);
  return __builtin_bit_cast(bf16x8, f);
}
__device__ __forceinline__ bf16x8 lds_frag(unsigned a) {
  return __builtin_bit_cast(bf16x8, *(LAS u32x4*)(size_t)a);
}
__device__ __forceinline__ f32x4 mfma(bf16x8 a, bf16x8 b, f32x4 c) {
  return __builtin_amdgcn_mfma_f32_16x16x32_bf16(a, b, c, 0, 0, 0);
}
typedef __bf16 bf16x2 __attribute__((ext_vector_type(2)));
typedef float f32x2 __attribute__((ext_vector_type(2)));
__device__ __forceinline__ unsigned pk(float a, float b) {
  f32x2 v = {a, b};
  return __builtin_bit_cast(unsigned, __builtin_convertvector(v, bf16x2));
}
__device__ __forceinline__ float geglu(float gt, float up) {
  const float c1 = -2.302208198f, c2 = -0.1029432397f;
  float e = __builtin_amdgcn_exp2f(gt * __builtin_fmaf(c2, gt * gt, c1));
  return gt * __builtin_amdgcn_rcpf(1.0f + e) * up;
}
__device__ __forceinline__ float phi(float t) {
  return (t > 0.f ? t + 1.0f : __builtin_amdgcn_exp2f(t * 1.4426950409f)) + 1e-6f;
}
__device__ __forceinline__ bf16x8 mk_frag(unsigned a, unsigned b, unsigned c, unsigned d) {
  u32x4 v = {a, b, c, d};
  return __builtin_bit_cast(bf16x8, v);
}

constexpr int K1_WR = 98304;
constexpr int u_base(int i) { return i < 8 ? i * 12288 : K1_WR + 2 * 16384; }
constexpr int K1_GW = K1_WR + 2 * 16384 + 12288;

constexpr int k1_nW(int t) { return (t + 3 <= 17) ? 2 : 0; }
constexpr int k1_nX(int t) { return (t + 3 <= 8) ? 4 : 0; }
constexpr int k1_nU(int t) { return (t >= 7 && t <= 15) ? (t == 15 ? 7 : 6) : 0; }
constexpr int k1_nS(int t) { return (t == 8) ? 4 : 0; }
constexpr int k1_step_ops(int t) { return k1_nW(t) + k1_nX(t) + k1_nU(t) + k1_nS(t); }
constexpr int k1_allow(int t) {
  if (t == 0) return 12;
  if (t == 1) return 6 + k1_step_ops(0);
  if (t == 2) return k1_step_ops(0) + k1_step_ops(1);
  return k1_nU(t - 3) + k1_nS(t - 3) + k1_step_ops(t - 2) + k1_step_ops(t - 1);
}

__global__ __launch_bounds__(512, 2) void k1_front(Params P) {
  extern __shared__ __attribute__((aligned(16))) char smem[];
  const unsigned L0 = (unsigned)(size_t)smem;
  const int tid = threadIdx.x, l = tid & 63;
  const int w = __builtin_amdgcn_readfirstlane(tid >> 6);
  const int b = blockIdx.x, e = b & 7, idx = b >> 3;
  const int j = 4 * e + (idx & 3), p = idx >> 2;
  const int fr = l & 15, g = l >> 4, q = fr >> 2, pp = l & 3, r8 = l >> 3, cp = l & 7;

  if (tid == 0) *(unsigned*)(P.ws + WS_FLAG + (size_t)b * 256) = 0u;

  const unsigned vx = (unsigned)((32 * w + r8) * LD_X + ((cp ^ (r8 & 6)) << 4));
  const char* sx = P.x + (size_t)p * 256 * LD_X;
  const int fw = ((r8 >> 1) & 1) | ((w & 1) << 1);
  const unsigned vw = (unsigned)((8 * w + r8) * LD_W + (((cp >> 1) ^ fw) << 5) + ((cp & 1) << 4));
  const char* sg = P.wg + (size_t)j * 256;
  const char* su = P.wu + (size_t)j * 256;
  auto stage_x = [&](int s) {
#pragma unroll
    for (int i = 0; i < 4; ++i)
      glds16(L0 + (s % 3) * 32768 + w * 4096 + i * 1024, vx, sx + s * 128 + i * 8 * LD_X);
  };
  auto stage_w = [&](int s) {
    const size_t o = (size_t)(s % 9) * 64 * LD_W + (s / 9) * 128;
    const unsigned d = L0 + K1_WR + (s & 3) * 16384 + w * 1024;
    glds16(d, vw, sg + o);
    glds16(d + 8192, vw, su + o);
  };
  auto stage_u = [&](int s) {
    const unsigned d = L0 + u_base(s);
#pragma unroll
    for (int i = 0; i < 4; ++i) {
      const int f = (i & 1) | ((w & 1) << 1);
      const int dd = ((l & 31) - 8 * f) & 31;
      int sgm = dd / 10, wi = dd - sgm * 10;
      if (sgm > 2) { sgm = 2; wi = 8; }
      if (wi > 8) wi = 8;
      const int col = (sgm == 0 ? 576 : (sgm == 1 ? 1152 : 0)) + 18 * j + 2 * wi;
      const unsigned vo = (unsigned)((8 * w + (l >> 5)) * LD_QKV + col * 2);
      glds4(d + (8 * w + 2 * i) * 128, vo, P.wqkv + (size_t)(64 * s + 2 * i) * LD_QKV);
    }
    const unsigned vl = (unsigned)((8 * w + (r8 ^ ((w & 1) << 2))) * LD_W + cp * 4);
    const size_t o = (size_t)s * 64 * LD_W + (4096 + 16 * j) * 2;
    glds4(d + 8192 + w * 256, vl, P.wg + o);
    glds4(d + 8192 + 2048 + w * 256, vl, P.wu + o);
  };

  stage_x(0); stage_w(0); stage_x(1); stage_w(1); stage_x(2); stage_w(2);

  const int fl = ((q >> 1) & 1) | ((g & 1) << 1);
  unsigned rb[4];
#pragma unroll
  for (int t = 0; t < 4; ++t) rb[t] = L0 + K1_WR + (8 * g + q) * 128 + ((t ^ fl) << 5) + 8 * pp;
  const unsigned xr0 = L0 + w * 4096 + fr * 128 + ((g ^ (fr & 6)) << 4);
  const unsigned xr1 = L0 + w * 4096 + fr * 128 + (((4 + g) ^ (fr & 6)) << 4);

  bf16x8 xa[18][2];
  f32x4 ag[4][2], au[4][2];
  char* hm = P.ws + WS_HMAIN + ((size_t)(16 * p + 2 * w) * 128 + 4 * j) * 1024 + l * 16;

  sfor<0, 18>([&](auto T) {
    constexpr int t = T.value, s = t % 9;
    wait_vm<k1_allow(t)>();
    bar();
    if constexpr (t + 3 <= 17) stage_w(t + 3);
    if constexpr (t <= 8) {
      constexpr int xo = (t % 3) * 32768;
      xa[2 * t][0] = lds_frag(xr0 + xo); xa[2 * t][1] = lds_frag(xr0 + xo + 2048);
      xa[2 * t + 1][0] = lds_frag(xr1 + xo); xa[2 * t + 1][1] = lds_frag(xr1 + xo + 2048);
      if constexpr (t + 3 <= 8) {
        asm volatile("" ::"v"(xa[2 * t][0]), "v"(xa[2 * t][1]), "v"(xa[2 * t + 1][0]), "v"(xa[2 * t + 1][1]));
        stage_x(t + 3);
      }
    }
    if constexpr (t >= 7 && t <= 15) stage_u(t - 7);
    if constexpr (t == 15) {
      int dw = 36 * w + l; dw = dw > 287 ? 287 : dw;
      glds4(L0 + K1_GW + w * 144, (unsigned)dw * 4, P.gw);
    }
    constexpr int wo = (t & 3) * 16384;
#pragma unroll
    for (int sub = 0; sub < 2; ++sub) {
#pragma unroll
      for (int tt = 0; tt < 4; ++tt) {
        const bf16x8 fg = tr_frag(rb[tt] + wo + sub * 4096, rb[tt] + wo + sub * 4096 + 512);
        const bf16x8 fu = tr_frag(rb[tt] + wo + 8192 + sub * 4096, rb[tt] + wo + 8192 + sub * 4096 + 512);
#pragma unroll
        for (int rt = 0; rt < 2; ++rt) {
          const f32x4 z = {0.f, 0.f, 0.f, 0.f};
          ag[tt][rt] = mfma(fg, xa[2 * s + sub][rt], (s == 0 && sub == 0) ? z : ag[tt][rt]);
          au[tt][rt] = mfma(fu, xa[2 * s + sub][rt], (s == 0 && sub == 0) ? z : au[tt][rt]);
        }
      }
    }
    if constexpr (s == 8) {
      constexpr int c = t / 9;
#pragma unroll
      for (int rt = 0; rt < 2; ++rt)
#pragma unroll
        for (int kb = 0; kb < 2; ++kb) {
          u32x4 v;
          const f32x4 g0 = ag[2 * kb][rt], u0 = au[2 * kb][rt], g1 = ag[2 * kb + 1][rt], u1 = au[2 * kb + 1][rt];
          v.x = pk(geglu(g0[0], u0[0]), geglu(g0[1], u0[1]));
          v.y = pk(geglu(g0[2], u0[2]), geglu(g0[3], u0[3]));
          v.z = pk(geglu(g1[0], u1[0]), geglu(g1[1], u1[1]));
          v.w = pk(geglu(g1[2], u1[2]), geglu(g1[3], u1[3]));
          *(u32x4*)(hm + (size_t)rt * 128 * 1024 + (2 * c + kb) * 1024) = v;
        }
    }
    asm volatile("s_waitcnt lgkmcnt(0)" ::: "memory");
  });

  wait_vm<4>();
  bar();
  const unsigned ub = L0 + (8 * g + q) * 128;
  const unsigned uK = ub + ((0 + 8 * pp + 32 * fl) & 127);
  const unsigned uV = ub + ((40 + 8 * pp + 32 * fl) & 127);
  const unsigned uQ = ub + ((80 + 8 * pp + 32 * fl) & 127);
  const unsigned uR = ub + (((pp == 3 ? 120 : 32 + 40 * pp) + 32 * fl) & 127);
  const unsigned uL0 = L0 + 8192 + (8 * g + (4 * (g & 1)) + q) * 32 + 8 * pp;
  const unsigned uL1 = L0 + 8192 + (8 * g + (4 ^ (4 * (g & 1))) + q) * 32 + 8 * pp;
  const unsigned gwr = L0 + K1_GW + g * 16;
  f32x4 cG[2], cU[2], cQ[2], cK[2], cV[2], cR[2];
  sfor<0, 18>([&](auto KK) {
    constexpr int kk = KK.value, uo = u_base(kk >> 1), sub = kk & 1;
    const f32x4 z = {0.f, 0.f, 0.f, 0.f};
    const bf16x8 fG = tr_frag(uL0 + uo + sub * 1024, uL1 + uo + sub * 1024);
    const bf16x8 fU = tr_frag(uL0 + uo + 2048 + sub * 1024, uL1 + uo + 2048 + sub * 1024);
    const bf16x8 fK = tr_frag(uK + uo + sub * 4096, uK + uo + sub * 4096 + 512);
    const bf16x8 fV = tr_frag(uV + uo + sub * 4096, uV + uo + sub * 4096 + 512);
    const bf16x8 fQ = tr_frag(uQ + uo + sub * 4096, uQ + uo + sub * 4096 + 512);
    bf16x8 fR = tr_frag(uR + uo + sub * 4096, uR + uo + sub * 4096 + 512);
    const bf16x8 fW = lds_frag(gwr + kk * 64);
    fR = (fr == 12) ? fW : fR;
#pragma unroll
    for (int rt = 0; rt < 2; ++rt) {
      cG[rt] = mfma(fG, xa[kk][rt], kk == 0 ? z : cG[rt]);
      cU[rt] = mfma(fU, xa[kk][rt], kk == 0 ? z : cU[rt]);
      cQ[rt] = mfma(fQ, xa[kk][rt], kk == 0 ? z : cQ[rt]);
      cK[rt] = mfma(xa[kk][rt], fK, kk == 0 ? z : cK[rt]);
      cV[rt] = mfma(xa[kk][rt], fV, kk == 0 ? z : cV[rt]);
      cR[rt] = mfma(xa[kk][rt], fR, kk == 0 ? z : cR[rt]);
    }
  });
#pragma unroll
  for (int rt = 0; rt < 2; ++rt) {
    const size_t o = ((size_t)(16 * p + 2 * w + rt) * 32 + j) * 512 + l * 8;
    u32x2 hv = {pk(geglu(cG[rt][0], cU[rt][0]), geglu(cG[rt][1], cU[rt][1])),
                pk(geglu(cG[rt][2], cU[rt][2]), geglu(cG[rt][3], cU[rt][3]))};
    *(u32x2*)(P.ws + WS_HLEFT + o) = hv;
    u32x2 qv = {pk(phi(cQ[rt][0]), phi(cQ[rt][1])), pk(phi(cQ[rt][2]), phi(cQ[rt][3]))};
    *(u32x2*)(P.ws + WS_PQM + o) = qv;
  }
  float pr[2][4];
#pragma unroll
  for (int rt = 0; rt < 2; ++rt)
#pragma unroll
    for (int r = 0; r < 4; ++r) pr[rt][r] = phi(cR[rt][r]);
#pragma unroll
  for (int rt = 0; rt < 2; ++rt) {
    u32x4 v;
#pragma unroll
    for (int r = 0; r < 4; ++r) v[r] = pk(pr[rt][r], __shfl_down(pr[rt][r], 1));
    const int row = 256 * p + 32 * w + 16 * rt + 4 * g;
    if (fr == 8) *(u32x4*)(P.ws + WS_PQR + ((size_t)j * 2048 + row) * 4) = v;
    if (fr == 12 && (j & 7) == 0) {
      f32x4 gv;
#pragma unroll
      for (int r = 0; r < 4; ++r)
        gv[r] = __builtin_amdgcn_rcpf(1.0f + __builtin_amdgcn_exp2f(-1.4426950409f * cR[rt][r]));
      *(f32x4*)(P.ws + WS_G + ((size_t)(j >> 3) * 2048 + row) * 4) = gv;
    }
  }
  {
    const bf16x8 aK = mk_frag(pk(phi(cK[0][0]), phi(cK[0][1])), pk(phi(cK[0][2]), phi(cK[0][3])),
                              pk(phi(cK[1][0]), phi(cK[1][1])), pk(phi(cK[1][2]), phi(cK[1][3])));
    const bf16x8 bV = mk_frag(pk(cV[0][0], cV[0][1]), pk(cV[0][2], cV[0][3]),
                              pk(cV[1][0], cV[1][1]), pk(cV[1][2], cV[1][3]));
    const bool isk = fr < 2, isv = (fr & 14) == 4, one = (fr & 3) == 2;
    u32x4 ra, rbv;
    ra.x = pk(pr[0][0], pr[0][1]); ra.y = pk(pr[0][2], pr[0][3]);
    ra.z = pk(pr[1][0], pr[1][1]); ra.w = pk(pr[1][2], pr[1][3]);
    rbv.x = pk(cR[0][0], cR[0][1]); rbv.y = pk(cR[0][2], cR[0][3]);
    rbv.z = pk(cR[1][0], cR[1][1]); rbv.w = pk(cR[1][2], cR[1][3]);
    const unsigned fill = one ? 0x3F803F80u : 0u;
#pragma unroll
    for (int i = 0; i < 4; ++i) { ra[i] = isk ? ra[i] : 0u; rbv[i] = isv ? rbv[i] : fill; }
    const bf16x8 aR = __builtin_bit_cast(bf16x8, ra), bR = __builtin_bit_cast(bf16x8, rbv);
    const f32x4 z = {0.f, 0.f, 0.f, 0.f};
    const unsigned sc = L0 + K1_WR + w * 4096 + l * 16;
    *(LAS f32x4*)(size_t)(sc) = mfma(aK, bV, z);
    *(LAS f32x4*)(size_t)(sc + 1024) = mfma(aR, bV, z);
    *(LAS f32x4*)(size_t)(sc + 2048) = mfma(aK, bR, z);
    *(LAS f32x4*)(size_t)(sc + 3072) = mfma(aR, bR, z);
  }
  wait_lds_bar();
  if (tid < 256) {
    f32x4 s = *(LAS f32x4*)(size_t)(L0 + K1_WR + tid * 16);
#pragma unroll
    for (int i = 1; i < 8; ++i) s += *(LAS f32x4*)(size_t)(L0 + K1_WR + i * 4096 + tid * 16);
    *(f32x4*)(P.ws + WS_KVP + ((size_t)p * 32 + j) * 4096 + tid * 16) = s;
  }
}

constexpr int K2_NS = 6;
constexpr int K2_KVF = K2_NS * 24576;
constexpr int K2_DB = 5;
constexpr int K2_DA = 5;
constexpr int k2_nB(int s) { return (s <= 20) ? 3 : 0; }
constexpr int k2_nA(int s) { return s < 16 ? 2 : (s < 18 ? 4 : 0); }
constexpr int k2_allow(int t) {
  int n = 0;
  for (int s = t + 1; s < t + K2_DB; ++s) n += k2_nB(s) + k2_nA(s);
  return n;
}

__device__ __forceinline__ void ld16(u32x4& v, unsigned voff, const char* sb) {
  asm volatile("global_load_dwordx4 %0, %1, %2" : "=v"(v) : "v"(voff), "s"(sb));
}
__device__ __forceinline__ void ld8(u32x2& v, unsigned voff, const char* sb) {
  asm volatile("global_load_dwordx2 %0, %1, %2" : "=v"(v) : "v"(voff), "s"(sb));
}

__global__ __launch_bounds__(512, 2) void k2_back(Params P) {
  extern __shared__ __attribute__((aligned(16))) char smem[];
  const unsigned L0 = (unsigned)(size_t)smem;
  const int tid = threadIdx.x, l = tid & 63;
  const int w = __builtin_amdgcn_readfirstlane(tid >> 6);
  const int mw = w & 3, kg = w >> 2;
  const int b = blockIdx.x, e = b & 7, idx = b >> 3;
  const int ks = e >> 1, m = 8 * (e & 1) + (idx >> 2), n = idx & 3;
  const int fr = l & 15, g = l >> 4, q = fr >> 2, pp = l & 3, r8 = l >> 3, cp = l & 7;
  const int rt0 = 8 * m + 2 * mw;

  const unsigned vcol = (unsigned)((((cp >> 1) ^ ((r8 >> 1) & 3)) << 5) + ((cp & 1) << 4));
  const unsigned vb = (unsigned)((8 * w + r8) * LD_X) + vcol;
  auto stage_b = [&](int s) {
    const unsigned d = L0 + (s % K2_NS) * 24576 + w * 3072;
    const char* sb;
    unsigned vo = vb;
    if (s < 16) sb = P.wd + (size_t)(1024 * ks + 64 * s) * LD_X;
    else if (s < 18) sb = P.wd + (size_t)(4096 + 128 * ks + 64 * (s - 16)) * LD_X;
    else if (s < 20) sb = P.wo + (size_t)(144 * ks + 64 * (s - 18)) * LD_X;
    else {
      sb = P.wo;
      int row = 144 * ks + 128 + 8 * w + r8; row = row > 575 ? 575 : row;
      vo = (unsigned)(row * LD_X) + vcol;
    }
    sb += 256 * n;
    glds16(d, vo, sb); glds16(d + 1024, vo, sb + 128); glds16(d + 2048, vo, sb + 256);
  };
  u32x4 A[K2_DA][2];
  const unsigned va = (unsigned)(((size_t)rt0 * 128 + 32 * ks + kg) * 1024 + l * 16);
  const unsigned vl = (unsigned)(((size_t)rt0 * 32 + 8 * ks + 2 * kg) * 512 + l * 8);
  u32x2 AL[2][2][2];
  auto load_a = [&](auto S) {
    constexpr int s = S.value;
    if constexpr (s < 16) {
      ld16(A[s % K2_DA][0], va, P.ws + WS_HMAIN + s * 2048);
      ld16(A[s % K2_DA][1], va, P.ws + WS_HMAIN + s * 2048 + 128 * 1024);
    } else if constexpr (s < 18) {
      const char* sb = P.ws + WS_HLEFT + (s - 16) * 2048;
      ld8(AL[s - 16][0][0], vl, sb); ld8(AL[s - 16][0][1], vl, sb + 512);
      ld8(AL[s - 16][1][0], vl, sb + 32 * 512); ld8(AL[s - 16][1][1], vl, sb + 32 * 512 + 512);
    }
  };

  {
    const char* kp = P.ws + WS_KVP + (size_t)(8 * ks + w) * 4096 + l * 16;
    f32x4 t[4];
#pragma unroll
    for (int i = 0; i < 4; ++i) t[i] = *(const f32x4*)(kp + i * 1024);
#pragma unroll
    for (int pn = 1; pn < 8; ++pn)
#pragma unroll
      for (int i = 0; i < 4; ++i) t[i] += *(const f32x4*)(kp + (size_t)pn * 32 * 4096 + i * 1024);
    u32x4 f0 = {pk(t[0][0], t[0][1]), pk(t[0][2], t[0][3]), pk(t[1][0], t[1][1]), pk(t[1][2], t[1][3])};
    u32x4 f1 = {pk(t[2][0], t[2][1]), pk(t[2][2], t[2][3]), pk(t[3][0], t[3][1]), pk(t[3][2], t[3][3])};
    *(LAS u32x4*)(size_t)(L0 + K2_KVF + w * 2048 + l * 16) = f0;
    *(LAS u32x4*)(size_t)(L0 + K2_KVF + w * 2048 + 1024 + l * 16) = f1;
  }
  float gv[2];
  u32x2 pqm[2][4];
  unsigned pqr[2][4];
#pragma unroll
  for (int rt = 0; rt < 2; ++rt) {
    const int row = 16 * (rt0 + rt) + fr;
    gv[rt] = *(const float*)(P.ws + WS_G + ((size_t)ks * 2048 + row) * 4);
#pragma unroll
    for (int i = 0; i < 4; ++i) {
      const int h = 8 * ks + 4 * kg + i;
      pqm[rt][i] = *(const u32x2*)(P.ws + WS_PQM + ((size_t)(rt0 + rt) * 32 + h) * 512 + l * 8);
      const unsigned r = *(const unsigned*)(P.ws + WS_PQR + ((size_t)h * 2048 + row) * 4);
      pqr[rt][i] = g == 0 ? r : 0u;
    }
  }
  asm volatile("s_waitcnt vmcnt(0)" ::: "memory");
#pragma unroll
  for (int rt = 0; rt < 2; ++rt) {
    asm volatile("" : "+v"(gv[rt]));
#pragma unroll
    for (int i = 0; i < 4; ++i) asm volatile("" : "+v"(pqm[rt][i]), "+v"(pqr[rt][i]));
  }

  sfor<0, K2_DB>([&](auto S) { stage_b(S.value); load_a(S); });

  const int r7 = 4 * (g & 1) + q, fl = (r7 >> 1) & 3;
  unsigned rb[4];
#pragma unroll
  for (int i = 0; i < 4; ++i)
    rb[i] = L0 + (4 * kg + (g >> 1)) * 3072 + ((n + i) >> 2) * 1024 + r7 * 128 + ((((n + i) & 3) ^ fl) << 5) + 8 * pp;

  f32x4 acc[9][2];
  sfor<0, 18>([&](auto T) {
    constexpr int t = T.value;
    bf16x8 a0, a1;
    if constexpr (t < 16) {
      asm volatile("s_waitcnt vmcnt(%2)" : "+v"(A[t % K2_DA][0]), "+v"(A[t % K2_DA][1]) : "i"(k2_allow(t)) : "memory");
      a0 = __builtin_bit_cast(bf16x8, A[t % K2_DA][0]); a1 = __builtin_bit_cast(bf16x8, A[t % K2_DA][1]);
    } else {
      asm volatile("s_waitcnt vmcnt(%4)"
                   : "+v"(AL[t - 16][0][0]), "+v"(AL[t - 16][0][1]), "+v"(AL[t - 16][1][0]), "+v"(AL[t - 16][1][1])
                   : "i"(k2_allow(t)) : "memory");
      a0 = mk_frag(AL[t - 16][0][0].x, AL[t - 16][0][0].y, AL[t - 16][0][1].x, AL[t - 16][0][1].y);
      a1 = mk_frag(AL[t - 16][1][0].x, AL[t - 16][1][0].y, AL[t - 16][1][1].x, AL[t - 16][1][1].y);
    }
    bar();
    if constexpr (t + K2_DB <= 20) stage_b(t + K2_DB);
    load_a(ic<t + K2_DA>{});
    constexpr int so = (t % K2_NS) * 24576;
#pragma unroll
    for (int c = 0; c < 9; ++c) {
      const unsigned ad = rb[c & 3] + so + (c >> 2) * 1024;
      const bf16x8 f = tr_frag(ad, ad + 6144);
      const f32x4 z = {0.f, 0.f, 0.f, 0.f};
      acc[c][0] = mfma(f, a0, t == 0 ? z : acc[c][0]);
      acc[c][1] = mfma(f, a1, t == 0 ? z : acc[c][1]);
    }
    asm volatile("s_waitcnt lgkmcnt(0)" ::: "memory");
  });

#pragma unroll
  for (int c = 0; c < 9; ++c) { acc[c][0] *= (1.0f - gv[0]); acc[c][1] *= (1.0f - gv[1]); }
  wait_vm<0>();
  bar();
#pragma unroll
  for (int i = 0; i < 4; ++i) {
    const int hh = 4 * kg + i;
    const bf16x8 f0 = lds_frag(L0 + K2_KVF + hh * 2048 + l * 16);
    const bf16x8 f1 = lds_frag(L0 + K2_KVF + hh * 2048 + 1024 + l * 16);
    bf16x8 ob[2];
#pragma unroll
    for (int rt = 0; rt < 2; ++rt) {
      const f32x4 z = {0.f, 0.f, 0.f, 0.f};
      const bf16x8 pq = mk_frag(pqm[rt][i].x, pqm[rt][i].y, pqr[rt][i], 0u);
      const f32x4 n0 = mfma(f0, pq, z), n1 = mfma(f1, pq, z);
      const float sc = gv[rt] * __builtin_amdgcn_rcpf(n1[2] + 1e-6f);
      const unsigned rem = pk(n1[0] * sc, n1[1] * sc);
      ob[rt] = mk_frag(pk(n0[0] * sc, n0[1] * sc), pk(n0[2] * sc, n0[3] * sc), g == 1 ? rem : 0u, 0u);
    }
    const int r1 = 18 * hh + 4 * g + q, r2 = 18 * hh + 16 + q;
    const unsigned a1 = L0 + (r1 >> 3) * 3072 + (r1 & 7) * 128 + (((r1 >> 1) & 3) << 5) + 8 * pp;
    const unsigned a2 = L0 + (r2 >> 3) * 3072 + (r2 & 7) * 128 + (((r2 >> 1) & 3) << 5) + 8 * pp;
#pragma unroll
    for (int c = 0; c < 9; ++c) {
      const int gs = n + c;
      const unsigned x = (unsigned)((gs & 3) << 5), o = (unsigned)((gs >> 2) * 1024);
      const bf16x8 f = tr_frag(((a1 - L0) ^ x) + L0 + o, ((a2 - L0) ^ x) + L0 + o);
      acc[c][0] = mfma(f, ob[0], acc[c][0]);
      acc[c][1] = mfma(f, ob[1], acc[c][1]);
    }
  }

  wait_lds_bar();
  f32x4 r[9];
#pragma unroll
  for (int c = 0; c < 9; ++c) {
    *(LAS f32x4*)(size_t)(L0 + w * 9216 + c * 1024 + l * 16) = kg ? acc[c][0] : acc[c][1];
    r[c] = kg ? acc[c][1] : acc[c][0];
  }
  wait_lds_bar();
#pragma unroll
  for (int c = 0; c < 9; ++c) r[c] += *(LAS f32x4*)(size_t)(L0 + (w ^ 4) * 9216 + c * 1024 + l * 16);

  const int tile = 4 * m + n, rtl = 2 * mw + kg;
  const char* slab = P.ws + WS_SLAB + (size_t)tile * 16 * 18432;
  const unsigned sub = (unsigned)(rtl * 2304 + (4 * g + pp) * 16);
  if (q != ks) {
    const unsigned vo = (unsigned)((q * 4 + ks) * 18432) + sub;
#pragma unroll
    for (int c = 0; c < 9; ++c)
      asm volatile("global_store_dwordx4 %0, %1, %2 offset:%3 sc1\n\ts_nop 1" ::"v"(vo), "v"(r[c]), "s"(slab), "i"(c * 256) : "memory");
  }
  asm volatile("s_waitcnt vmcnt(0)" ::: "memory");
  bar();
  unsigned* flags = (unsigned*)(P.ws + WS_FLAG);
  if (tid == 0) __hip_atomic_store(flags + (tile * 4 + ks) * 64, 1u, __ATOMIC_RELAXED, __HIP_MEMORY_SCOPE_AGENT);
  if (w == 0) {
    const unsigned* f = flags + (tile * 4 + (l & 3)) * 64;
    for (unsigned spins = 0; spins < (1u << 22); ++spins) {
      const unsigned v = __hip_atomic_load(f, __ATOMIC_RELAXED, __HIP_MEMORY_SCOPE_AGENT);
      if (__all(v == 1u)) break;
      __builtin_amdgcn_s_sleep(1);
    }
  }
  bar();
  {
    f32x4 in[9];
    const unsigned vo = (unsigned)((ks * 4 + (q == ks ? (ks ^ 1) : q)) * 18432) + sub;
#pragma unroll
    for (int c = 0; c < 9; ++c)
      asm volatile("global_load_dwordx4 %0, %1, %2 offset:%3 sc1" : "=v"(in[c]) : "v"(vo), "s"(slab), "i"(c * 256));
    asm volatile("s_waitcnt vmcnt(0)"
                 : "+v"(in[0]), "+v"(in[1]), "+v"(in[2]), "+v"(in[3]), "+v"(in[4]), "+v"(in[5]), "+v"(in[6]), "+v"(in[7]), "+v"(in[8])
                 :: "memory");
    char* op = P.out + (size_t)(128 * m + 16 * rtl + 4 * ks + pp) * LD_X + (144 * n + 4 * g) * 2;
#pragma unroll
    for (int c = 0; c < 9; ++c) {
      f32x4 v = (q == ks) ? r[c] : in[c];
#pragma unroll
      for (int i = 0; i < 4; ++i) {
        v[i] += __shfl_xor(v[i], 4);
        v[i] += __shfl_xor(v[i], 8);
      }
      if (q == 0) { u32x2 o = {pk(v[0], v[1]), pk(v[2], v[3])}; *(u32x2*)(op + c * 32) = o; }
    }
  }
}

}

extern "C" void kernel_launch(void* const* d_in, const int* in_sizes, int n_in, void* d_out, int out_size,
                              void* d_ws, size_t ws_size, hipStream_t stream) {
  Params P;
  P.x = (const char*)d_in[0]; P.wqkv = (const char*)d_in[1]; P.wo = (const char*)d_in[2];
  P.wg = (const char*)d_in[4]; P.wu = (const char*)d_in[5]; P.wd = (const char*)d_in[6];
  P.gw = (const char*)d_in[7]; P.out = (char*)d_out; P.ws = (char*)d_ws;
  (void)hipFuncSetAttribute((const void*)k1_front, hipFuncAttributeMaxDynamicSharedMemorySize, 163840);
  (void)hipFuncSetAttribute((const void*)k2_back, hipFuncAttributeMaxDynamicSharedMemorySize, 163840);
  k1_front<<<256, 512, 163840, stream>>>(P);
  k2_back<<<256, 512, 163840, stream>>>(P);
}
```

```cpp
#include <hip/hip_runtime.h>
#include <hip/hip_bf16.h>

#define LAS __attribute__((address_space(3)))
typedef short s16x4 __attribute__((ext_vector_type(4)));
typedef short s16x8 __attribute__((ext_vector_type(8)));
typedef __bf16 bf16x8 __attribute__((ext_vector_type(8)));
typedef __bf16 bf16x2 __attribute__((ext_vector_type(2)));
typedef float f32x2 __attribute__((ext_vector_type(2)));
typedef float f32x4 __attribute__((ext_vector_type(4)));
typedef float f32x16 __attribute__((ext_vector_type(16)));
typedef unsigned u32x4 __attribute__((ext_vector_type(4)));
typedef unsigned u32x2 __attribute__((ext_vector_type(2)));
typedef unsigned u32x3 __attribute__((ext_vector_type(3)));
typedef unsigned long long u64;

namespace {
#define DIAG_INIT()
#define DIAG_END(k)
#define STAMP(i)
#define RDEPTH 4
#define NTG " nt"
#define NTT ""
#define NTW ""
#define NTX ""
#define TWON 1
#define TXON 1
#define ROT1 0
#define CPN 0

constexpr int LD_X = 1152;
constexpr int LD_W = 9216;
constexpr int LD_QKV = 3456;

constexpr unsigned WS_EPOCH = 0;
constexpr unsigned WS_FLAG = 4096;
constexpr unsigned WS_H = 327680;
constexpr unsigned WS_PQM = WS_H + 128u * 144 * 1024;
constexpr unsigned WS_PQR = WS_PQM + 128u * 32 * 512;
constexpr unsigned WS_KVP = WS_PQR + 32u * 2048 * 8;
constexpr unsigned WS_KVT = WS_KVP + 8u * 32 * 1536;
constexpr unsigned WS_PK = WS_KVT + 32u * 1024;
constexpr unsigned WS_DP = WS_PK + 32u * 120 * 1024;
constexpr unsigned DP_PITCH = 384u * 1024;
constexpr unsigned WS_SLAB = WS_DP + 16u * DP_PITCH;
constexpr unsigned WS_END = WS_SLAB + 64u * 16 * 9216;

struct Params {
  const char* x; const char* wqkv; const char* wo; const char* wg; const char* wu; const char* wd;
  const char* gw; char* out; char* ws;
};

__device__ __forceinline__ u32x4 mk_srd(const void* p, unsigned bytes) {
  const u64 a = (u64)p;
  u32x4 s = {(unsigned)a, (unsigned)(a >> 32) & 0xffffu, bytes, 0x00020000u};
  return s;
}
#define DSET(lds, LO, sb, SO) asm volatile("s_add_u32 m0, %1, %3\n\ts_add_u32 %0, %2, %4" : "=s"(dso) : "s"(lds), "s"(sb), "i"(LO), "i"(SO) : "scc")
#define DSET0(lds, LO, SO) asm volatile("s_add_u32 m0, %1, %2\n\ts_mov_b32 %0, %3" : "=s"(dso) : "s"(lds), "i"(LO), "i"(SO) : "scc")
#define DOP(fl, voff, srd, IM) asm volatile("buffer_load_dwordx4 %0, %1, %2 offen offset:%3" fl " lds" ::"v"(voff), "s"(srd), "s"(dso), "i"(IM))
#define BLD(op, dst, voff, sb, SO) do { unsigned t_; \
    asm volatile("s_add_u32 %1, %4, %5\n\tbuffer_load_" op " %0, %2, %3, %1 offen sc1" \
                 : "=v"(dst), "=&s"(t_) : "v"(voff), "s"(rws), "s"(sb), "i"(SO) : "scc"); } while (0)
#define BST(op, val, voff, sb, SO) do { unsigned t_; \
    asm volatile("s_add_u32 %0, %4, %5\n\tbuffer_store_" op " %1, %2, %3, %0 offen sc1\n\ts_nop 1" \
                 : "=&s"(t_) : "v"(val), "v"(voff), "s"(rws), "s"(sb), "i"(SO) : "scc", "memory"); } while (0)
#define BSTI(op, val, voff, sb, IM) asm volatile("buffer_store_" op " %0, %1, %2, %3 offen offset:%4 sc1" ::"v"(val), "v"(voff), "s"(rws), "s"(sb), "i"(IM) : "memory")
#define BSTI4(val, voff, sb, IM) asm volatile("buffer_store_dwordx4 %0, %1, %2, %3 offen offset:%4 sc1\n\ts_nop 1" ::"v"(val), "v"(voff), "s"(rws), "s"(sb), "i"(IM) : "memory")
#define BLDI(op, dst, voff, srd, sb, IM) asm volatile("buffer_load_" op " %0, %1, %2, %3 offen offset:%4 sc1" : "=v"(dst) : "v"(voff), "s"(srd), "s"(sb), "i"(IM))

__device__ __forceinline__ void wait_lds_bar() {
  asm volatile("s_waitcnt lgkmcnt(0)" ::: "memory");
  __builtin_amdgcn_s_barrier();
  asm volatile("" ::: "memory");
}
__device__ __forceinline__ s16x4 tr4(unsigned a) { return __builtin_amdgcn_ds_read_tr16_b64_v4i16((LAS s16x4*)(size_t)a); }
__device__ __forceinline__ bf16x8 tr_frag(unsigned a0, unsigned a1) {
  const s16x4 lo = tr4(a0), hi = tr4(a1);
  return __builtin_bit_cast(bf16x8, (s16x8)__builtin_shufflevector(lo, hi, 0, 1, 2, 3, 4, 5, 6, 7));
}
__device__ __forceinline__ bf16x8 lds_frag(unsigned a) { return __builtin_bit_cast(bf16x8, *(LAS u32x4*)(size_t)a); }
__device__ __forceinline__ f32x4 mfma(bf16x8 a, bf16x8 b, f32x4 c) { return __builtin_amdgcn_mfma_f32_16x16x32_bf16(a, b, c, 0, 0, 0); }
__device__ __forceinline__ f32x16 mfma32(bf16x8 a, bf16x8 b, f32x16 c) { return __builtin_amdgcn_mfma_f32_32x32x16_bf16(a, b, c, 0, 0, 0); }
__device__ __forceinline__ unsigned pk(float a, float b) {
  f32x2 v = {a, b};
  return __builtin_bit_cast(unsigned, __builtin_convertvector(v, bf16x2));
}
__device__ __forceinline__ unsigned pk2(f32x2 v) { return __builtin_bit_cast(unsigned, __builtin_convertvector(v, bf16x2)); }
__device__ __forceinline__ f32x2 ex2(f32x2 v) { f32x2 r = {__builtin_amdgcn_exp2f(v.x), __builtin_amdgcn_exp2f(v.y)}; return r; }
__device__ __forceinline__ f32x2 rc2(f32x2 v) { f32x2 r = {__builtin_amdgcn_rcpf(v.x), __builtin_amdgcn_rcpf(v.y)}; return r; }
__device__ __forceinline__ bf16x8 mk_frag(unsigned a, unsigned b, unsigned c, unsigned d) {
  u32x4 v = {a, b, c, d};
  return __builtin_bit_cast(bf16x8, v);
}
template <int N> __device__ __forceinline__ void check_flag(u32x2& f, unsigned voff, u32x4 srd, u64 tag) {
  unsigned cnt;
  asm volatile("s_waitcnt vmcnt(%5)\n\t"
               "s_mov_b32 %1, 0\n"
               "1:\n\t"
               "v_cmp_ne_u64 vcc, %0, %4\n\t"
               "s_cbranch_vccz 2f\n\t"
               "s_add_u32 %1, %1, 1\n\t"
               "s_cmp_gt_u32 %1, 0x40000\n\t"
               "s_cbranch_scc1 2f\n\t"
               "s_sleep 1\n\t"
               "buffer_load_dwordx2 %0, %2, %3, 0 offen sc1\n\t"
               "s_waitcnt vmcnt(0)\n\t"
               "s_branch 1b\n"
               "2:"
               : "+v"(f), "=&s"(cnt) : "v"(voff), "s"(srd), "s"(tag), "i"(N) : "vcc", "scc", "memory");
}
__device__ __forceinline__ void ld_flag(u32x2& f, unsigned voff, u32x4 srd) {
  asm volatile("buffer_load_dwordx2 %0, %1, %2, 0 offen sc1" : "=v"(f) : "v"(voff), "s"(srd));
}
__device__ __forceinline__ f32x4 add4(f32x4 a, f32x4 b) {
  float d0, d1, d2, d3;
  asm volatile("v_add_f32 %0, %4, %8\n\tv_add_f32 %1, %5, %9\n\tv_add_f32 %2, %6, %10\n\tv_add_f32 %3, %7, %11"
               : "=&v"(d0), "=&v"(d1), "=&v"(d2), "=&v"(d3) : "v"(a[0]), "v"(a[1]), "v"(a[2]), "v"(a[3]), "v"(b[0]), "v"(b[1]), "v"(b[2]), "v"(b[3]));
  f32x4 r = {d0, d1, d2, d3};
  return r;
}
__device__ __forceinline__ f32x4 mul4(f32x4 a, float s) {
  float d0, d1, d2, d3;
  asm volatile("v_mul_f32 %0, %4, %8\n\tv_mul_f32 %1, %5, %8\n\tv_mul_f32 %2, %6, %8\n\tv_mul_f32 %3, %7, %8\n\ts_nop 1"
               : "=&v"(d0), "=&v"(d1), "=&v"(d2), "=&v"(d3) : "v"(a[0]), "v"(a[1]), "v"(a[2]), "v"(a[3]), "v"(s));
  f32x4 r = {d0, d1, d2, d3};
  return r;
}
__device__ __forceinline__ float sum4(float a) {
  a += __builtin_bit_cast(float, __builtin_amdgcn_update_dpp(0, __builtin_bit_cast(int, a), 0x124, 0xf, 0xf, false));
  a += __builtin_bit_cast(float, __builtin_amdgcn_update_dpp(0, __builtin_bit_cast(int, a), 0x128, 0xf, 0xf, false));
  return a;
}

constexpr int LEFT = 98304, SCR = 147456, KVF = 153600, RD = RDEPTH;

__global__ __launch_bounds__(512, 2) void fused_kernel(Params P) {
  extern __shared__ __attribute__((aligned(16))) char smem[];
  const unsigned L0 = (unsigned)(size_t)smem;
  const int tid = threadIdx.x, l = tid & 63;
  unsigned tw[4];
  {
    u64 pc_; unsigned sz_;
    asm volatile("s_getpc_b64 %0\nfk_code_a:\n\ts_mov_b32 %1, fk_code_b-fk_code_a" : "=s"(pc_), "=s"(sz_));
    unsigned o_ = (l < CPN ? l : (blockIdx.x >> 3) * 16 + (tid >> 6) * 2 + (l & 1)) * 128; o_ = o_ < sz_ ? o_ : 0u;
    asm volatile("global_load_dword %0, %1, %2" : "=v"(tw[0]) : "v"(o_), "s"(pc_));
  }
  const int w = __builtin_amdgcn_readfirstlane(tid >> 6);
  const int b = blockIdx.x, e = b & 7, idx = b >> 3;
  const int j = 4 * e + (idx & 3), p = idx >> 2;
  const int ks = e >> 1, m = 8 * (e & 1) + (idx >> 2), n = idx & 3;
  const int mw = w & 3, kg = w >> 2, rot = ROT1;
  const int r8 = l >> 3, cp = l & 7;
  DIAG_INIT();

  const u32x4 rx0 = mk_srd(P.x, 2048u * LD_X), rx1 = mk_srd(P.x + 8192, 2048u * LD_X - 8192), rx2 = mk_srd(P.x + 16384, 2048u * LD_X - 16384),
              rx3 = mk_srd(P.x + 24576, 2048u * LD_X - 24576);
  const u32x4 rg = mk_srd(P.wg, 576u * LD_W), ru = mk_srd(P.wu - 1024, 576u * LD_W + 1024), rws = mk_srd(P.ws, WS_END);
  const unsigned vx = (unsigned)((256 * p + 32 * w + r8) * LD_X + ((cp ^ (r8 & 6)) << 4));
  const unsigned vw = (unsigned)((8 * w + r8) * LD_W + j * 256 + ((cp ^ (((r8 >> 1) & 1) << 2)) << 4));
  unsigned va = (unsigned)l * 16, dso, aso;
  const unsigned Lw = L0 + w * 1024, Lw2 = L0 + w * 2048, Lw3 = L0 + w * 3072, Lx = L0 + w * 4096;
  unsigned s_h, s_h1, s_hl, s_pk, s_dp, s_slab, s_pq, s_pr, s_dsrc, s_ddst, s_lsrc, s_ldst, dA, dB, dC, dE, s_x1w, s_x1r, s_q9a, s_q9b, l_q9a, l_q9b;
  u32x4 rha, rhb, rdsrc;
  int du, dq, dr;
  const u64 smask = (u64)(0xFFFFu & ~(0xFu << (4 * ks))) * 0x0001000100010001ull;
  bf16x8 xa[36];
  bf16x8 fb[RD + 1];
  float ta[2][2], tb[2][2];
  f32x16 aw[2][2][2];
  unsigned hp[4], b2[8], a1[8], b1[8], qp[2];
  f32x16 cL, cT1, cT2, kv1, kv2;
  u32x4 pkr[5];
  unsigned pko[3], pkg[2], pkv[2];
  bool dpo[3];
  unsigned rb[2], xr, ut, uL0, uL1, qw, hso, t0, t1, t2, d0, d1;
  unsigned Lq;
  bool is28, isk, is0, pkh[2];
  u64 tag;
  const f32x16 Z16 = {0.f, 0.f, 0.f, 0.f, 0.f, 0.f, 0.f, 0.f, 0.f, 0.f, 0.f, 0.f, 0.f, 0.f, 0.f, 0.f};
  const f32x4 Z = {0.f, 0.f, 0.f, 0.f};
  u32x4 A[18][2], kvl[4][2];
  f32x4 kvs[2], acc[9][2], r[9], n0[2][2], n1[2][2], kvr[8], xr3[3];
  float gm[2];
  unsigned eo, vo;
  u32x3 in[6];
  unsigned xp[6];
  u32x4 pq4[5];
  u32x2 ef, fl, kx[8];
  unsigned vf, r2[3], oa[2], om[2][4][2], orem[2], vs, kfa;
  float gv[2], k2v;

#define GETLANE() ({ unsigned z_; asm volatile("v_lshrrev_b32 %0, 4, %1" : "=v"(z_) : "v"(va)); z_; })
#define PIN(a) asm volatile("" : "+v"(a))
#define PIN2(a, b) asm volatile("" : "+v"(a), "+v"(b))
#define PLAINBAR() do { asm volatile("" ::: "memory"); __builtin_amdgcn_s_barrier(); asm volatile("" ::: "memory"); } while (0)
#define KB(s) ((unsigned)((s) + rot >= 9 ? (s) + rot - 9 : (s) + rot))
#define STW_S(s, o) DSET(Lw2, o, KB((s) % 9) * (64 * LD_W), ((s) / 9) * 128)
#define STW(i) DOP(NTW, vw, ((i) ? ru : rg), (i) * 1024)
#define STX_S(s, o) DSET(Lx, o, KB(s) * 128, 0)
#define STX(i) DOP(NTX, vx, ((i) == 0 ? rx0 : (i) == 1 ? rx1 : (i) == 2 ? rx2 : rx3), (i) * 1024)
#define PKQ(i) (8 * (i) + w)
#define PKI(i) ({ const int q_ = 11 * p + PKQ(i); (PKQ(i) > 10 || q_ > 80) ? 117 : q_; })
#define PK_AD(i, z) do { const unsigned LZ_ = GETLANE(); const int pi_ = PKI(i), kq_ = pi_ / 9; \
    if constexpr ((z) == 0) { const unsigned c_ = 64 * (pi_ - 9 * kq_) + LZ_; t0 = c_ / 9; t1 = c_ - 9 * t0; \
      t0 = 64 * kq_ + (t0 & 48) + 4 * (t0 & 3) + ((t0 >> 2) & 3); PIN2(t0, t1); } \
    else { const unsigned sg_ = t1 / 3, pc_ = t1 - 3 * sg_; \
      pkh[i] = t1 == 2; pkg[i] = pi_ > 80 ? 0u : t0 * 2; \
      pko[i] = pi_ > 80 ? 0u : t0 * LD_QKV + ((sg_ == 0 ? 576 : (sg_ == 1 ? 1152 : 0)) + 18 * j) * 2 + (pc_ == 2 ? 20 : 16 * pc_); PIN2(pko[i], pkg[i]); } } while (0)
#define PK_LD(i) do { \
    if constexpr ((i) < 2) asm volatile("global_load_dwordx4 %0, %1, %2" NTG : "=v"(pkr[i]) : "v"(pko[i]), "s"(P.wqkv)); \
    else asm volatile("global_load_ushort %0, %1, %2" : "=v"(pkv[(i) & 1]) : "v"(pkg[(i) & 1]), "s"(P.gw)); } while (0)
#define PK_WAIT(nn) asm volatile("s_waitcnt vmcnt(%4)" : "+v"(pkr[0]), "+v"(pkr[1]), "+v"(pkv[0]), "+v"(pkv[1]) : "i"(nn))
#define PK_ST(i) do { pkr[i].x = pkh[i] ? pkv[i] : pkr[i].x; BST("dwordx4", pkr[i], va, (unsigned)((j * 120 + PKI(i)) * 1024), WS_PK); } while (0)
#define STQ_S(s, o) DSET(Lw, o, s_pk + KB(s) * 9216, 0)
#define STQ9_S(z) DSET(((z) ? l_q9b : l_q9a), 8192, ((z) ? s_q9b : s_q9a), 8192)
#define STL_S(i) do { if constexpr ((i) == 0) DSET(Lx, LEFT, s_pk + (KB(w) * 4 - w) * 1024, 81 * 1024); else DSET(L0 + (w & 3) * 1024, LEFT + 32768, s_pk + (KB(8) * 4 - (w & 4)) * 1024, 81 * 1024); } while (0)
#define STP(i) DOP(" sc1", va, rws, (i) * 1024)
#define DP_AD(i) do { const unsigned LZ_ = GETLANE(); const unsigned c_ = 64 * (i) + LZ_; unsigned r_ = __umul24(c_, 171u) >> 10; const unsigned ch_ = c_ - __umul24(r_, 6u); \
    r_ &= dC; t0 = __umul24(r_ + __umul24(dA, r_ >> dB), LD_X) + 16 * ch_; PIN(t0); } while (0)
#define GDMA(LO, voff, srd, so, IM) asm volatile("s_add_u32 m0, %0, %4\n\ts_nop 0\n\tbuffer_load_dwordx4 %1, %2, %3 offen offset:%5" NTG " lds" \
    ::"s"(Lx), "v"(voff), "s"(srd), "s"(so), "i"(49152 + (LO) - (IM)), "i"(IM) : "scc")
#define DP_LD(i) GDMA((i) * 1024, t0, rdsrc, s_dsrc, 0)
#define LP_AD() do { const unsigned LZ_ = GETLANE(); t0 = ((LZ_ >> 1) & 15) * LD_W + (LZ_ & 1) * 16; PIN(t0); } while (0)
#define LP_LD(i) asm volatile("s_add_u32 m0, %0, %4\n\ts_mov_b32 exec_hi, 0\n\tbuffer_load_dwordx4 %1, %2, %3 offen offset:%5" NTG " lds\n\ts_mov_b32 exec_hi, -1" \
    ::"s"(Lx), "v"(t0), "s"((i) ? ru : rg), "s"(s_lsrc), "i"(49152 + 3072 + (i) * 512 - (i) * 1024), "i"((i) * 1024) : "scc")
#define TW_LD(i) do { const unsigned z_ = GETLANE(); \
    if constexpr ((i) < 4) { const unsigned o_ = (72 * p + 9 * w + (z_ > 8 ? 8 : z_)) * LD_W + j * 256 + ((i) >> 1) * 128; \
      asm volatile("buffer_load_dword %0, %1, %2, 0 offen offset:%3" NTT : "=v"(tw[1 + ((i) & 1)]) : "v"(TWON ? o_ : 0u), "s"(((i) & 1) ? ru : rg), "i"(((i) & 1) * 1024)); } \
    else if constexpr ((i) % 10 < 7) { const unsigned o_ = (192 * ((i) / 10) + 24 * p + 3 * w + (z_ > 2 ? 2 : z_)) * LD_W + j * 256; \
      asm volatile("buffer_load_dword %0, %1, %2, 0 offen offset:%3" NTT : "=v"(tw[(i) % 10 - 4]) : "v"(TWON ? o_ : 0u), "s"(((i) % 10 == 6) ? ru : rg), "i"(((i) % 10 == 6) * 1024)); } \
    else { const unsigned o_ = (b * 8 + w) * LD_X + (3 * ((i) / 10) + (z_ > 2 ? 2 : z_)) * 128; \
      asm volatile("buffer_load_dword %0, %1, %2, 0 offen" : "=v"(tw[3]) : "v"(TXON ? o_ : 0u), "s"(rx0)); } } while (0)
#define TW_WAIT(nn) asm volatile("s_waitcnt vmcnt(%3)" : "+v"(tw[1]), "+v"(tw[2]), "+v"(tw[3]) : "i"(nn))
#define TW_WAIT2(nn) asm volatile("s_waitcnt vmcnt(%2)" : "+v"(tw[1]), "+v"(tw[2]) : "i"(nn))
#define DP_WAIT(nn) asm volatile("s_waitcnt vmcnt(%0)" ::"i"(nn) : "memory")
#define DP_RA(i) do { const unsigned LZ_ = GETLANE(); \
    if constexpr ((i) == 0) { d0 = Lx + 49152 + (((LZ_ >> 2) & 7) + (LZ_ >> 5) * dE) * 96 + 8 * (LZ_ & 3); d1 = d0 + (24 - dE) * 96; PIN2(d0, d1); } \
    else if constexpr ((i) == 3) { d0 = Lx + 49152 + 3072 + (LZ_ & 16) * 32 + (LZ_ >> 5) * 256 + (LZ_ & 15) * 8; d1 = d0 + 128; PIN2(d0, d1); } } while (0)
#define DP_RD(i) pkr[0] = __builtin_bit_cast(u32x4, tr_frag(d0 + ((i) % 3) * 32, d1 + ((i) % 3) * 32))
#define DP_ST(i) do { if constexpr ((i) < 3) BSTI4(pkr[0], va, s_ddst, (i) * 1024); else BSTI4(pkr[0], va, s_ldst, 0); } while (0)
#define TSB(v)
#define WAITBAR(nn, lg) do { TSB(dv0_); asm volatile("s_waitcnt vmcnt(%0) lgkmcnt(%1)" ::"i"(nn), "i"(lg) : "memory"); TSB(dv1_); __builtin_amdgcn_s_barrier(); asm volatile("" ::: "memory"); TSB(dv2_); } while (0)
#define WAITA(nn, t) asm volatile("s_waitcnt vmcnt(%2)" : "+v"(A[t][0]), "+v"(A[t][1]) : "i"(nn))
#define MARK() do { TSB(dv0_); TSB(dv1_); TSB(dv2_); } while (0)
#define NOPT()
#define SCHED() __builtin_amdgcn_sched_barrier(0)
#define RDX(s, o) do { PIN(xr); \
    xa[4 * (s)] = lds_frag(L0 + xr + (o)); xa[4 * (s) + 1] = lds_frag(L0 + (xr ^ 32) + (o)); \
    xa[4 * (s) + 2] = lds_frag(L0 + (xr ^ 64) + (o)); xa[4 * (s) + 3] = lds_frag(L0 + (xr ^ 96) + (o)); } while (0)
#define LWAIT(nn) __builtin_amdgcn_s_waitcnt(0xC07F | ((nn) << 8))
#define RDW(bf, tt, o) fb[bf] = tr_frag(rb[tt] + (o), rb[tt] + (o) + 512)
#define SHW0(d) do { rb[0] += (d); PIN(rb[0]); } while (0)
#define SHW1(d) do { rb[1] += (d); PIN(rb[1]); } while (0)
#define SHT(d) do { ut += (d); PIN(ut); } while (0)
#define MMW(bf, c, tt, mm, kk, first) aw[c][tt][mm] = mfma32(fb[bf], xa[kk], (first) ? Z16 : aw[c][tt][mm])
#define GS1(z, g0, g1) asm volatile("v_mul_f32 %0, 0xbdd2d3e8, %2\n\tv_mul_f32 %1, 0xbdd2d3e8, %3\n\tv_fma_f32 %0, %0, %2, %4\n\tv_fma_f32 %1, %1, %3, %4" \
    : "=&v"(ta[z][0]), "=&v"(ta[z][1]) : "v"(g0), "v"(g1), "s"(-2.302208198f))
#define GS2(z, g0, g1, u0, u1_) asm volatile("v_mul_f32 %0, %0, %4\n\tv_mul_f32 %1, %1, %5\n\tv_mul_f32 %2, %4, %6\n\tv_mul_f32 %3, %5, %7" \
    : "+v"(ta[z][0]), "+v"(ta[z][1]), "=&v"(tb[z][0]), "=&v"(tb[z][1]) : "v"(g0), "v"(g1), "v"(u0), "v"(u1_))
#define G1(c, tt, i) GS1((i) & 1, aw[c][tt][0][2 * (i)], aw[c][tt][0][2 * (i) + 1])
#define G2(c, tt, i) GS2((i) & 1, aw[c][tt][0][2 * (i)], aw[c][tt][0][2 * (i) + 1], aw[c][tt][1][2 * (i)], aw[c][tt][1][2 * (i) + 1])
#define GL1(i) GS1((i) & 1, cL[2 * (i)], cL[2 * (i) + 1])
#define GL2(i) GS2((i) & 1, cL[2 * (i)], cL[2 * (i) + 1], cL[8 + 2 * (i)], cL[9 + 2 * (i)])
#define G3(i) asm volatile("v_exp_f32 %0, %0\n\tv_exp_f32 %1, %1" : "+v"(ta[(i) & 1][0]), "+v"(ta[(i) & 1][1]))
#define G4(i) asm volatile("v_add_f32 %0, 1.0, %0\n\tv_add_f32 %1, 1.0, %1\n\tv_rcp_f32 %0, %0" : "+v"(ta[(i) & 1][0]), "+v"(ta[(i) & 1][1]))
#define G5(i) asm volatile("v_rcp_f32 %2, %2\n\tv_mul_f32 %1, %1, %3\n\tv_mul_f32 %2, %2, %4\n\tv_cvt_pk_bf16_f32 %0, %1, %2" \
    : "=v"(hp[(i) & 3]), "+v"(ta[(i) & 1][0]), "+v"(ta[(i) & 1][1]) : "v"(tb[(i) & 1][0]), "v"(tb[(i) & 1][1]))
#define H_ADDR() do { const unsigned LZ_ = GETLANE(); hso = ((LZ_ >> 4) & 1) * (144 * 1024) + (LZ_ >> 5) * 256 + (LZ_ & 15) * 16; PIN(hso); } while (0)
#define STH(c, tt, s) do { u32x4 v_ = {hp[0], hp[1], hp[2], hp[3]}; BSTI4(v_, hso, ((c) ? s_h1 : s_h), (tt) * 1024 + (s) * 512); } while (0)
#define ST_L() do { u32x4 v_ = {hp[0], hp[1], hp[2], hp[3]}; BSTI4(v_, hso, s_hl, 0); } while (0)
#define RDL(bf, o) fb[bf] = lds_frag(ut + (o))
#define RDT(bf, o) fb[bf] = tr_frag(ut + (o), ut + (o) + 144)
#define MML(bf, kk, first) cL = mfma32(fb[bf], xa[kk], (first) ? Z16 : cL)
#define MMT1(bf, kk, first) cT1 = mfma32(xa[kk], fb[bf], (first) ? Z16 : cT1)
#define MMT2(bf, kk, first) cT2 = mfma32(xa[kk], fb[bf], (first) ? Z16 : cT2)
#define PH1(c, i) asm volatile("v_mul_f32 %0, 0x3fb8aa3b, %2\n\tv_mul_f32 %1, 0x3fb8aa3b, %3\n\tv_min_f32 %0, 0, %0\n\tv_min_f32 %1, 0, %1" \
    : "=&v"(ta[0][0]), "=&v"(ta[0][1]) : "v"(c[2 * (i)]), "v"(c[2 * (i) + 1]))
#define PH2() asm volatile("v_exp_f32 %0, %0\n\tv_exp_f32 %1, %1" : "+v"(ta[0][0]), "+v"(ta[0][1]))
#define PH3(c, i) asm volatile("v_max_f32 %0, 0, %2\n\tv_max_f32 %1, 0, %3\n\tv_add_f32 %0, 0x358637bd, %0\n\tv_add_f32 %1, 0x358637bd, %1" \
    : "=&v"(tb[0][0]), "=&v"(tb[0][1]) : "v"(c[2 * (i)]), "v"(c[2 * (i) + 1]))
#define PH4(i) asm volatile("v_add_f32 %1, %1, %3\n\tv_add_f32 %2, %2, %4\n\tv_cvt_pk_bf16_f32 %0, %1, %2" \
    : "=v"(qp[(i) & 1]), "+v"(tb[0][0]), "+v"(tb[0][1]) : "v"(ta[0][0]), "v"(ta[0][1]))
#define Q3(i) do { b2[i] = pk(cT2[2 * (i)], cT2[2 * (i) + 1]); qp[(i) & 1] = is28 ? b2[i] : qp[(i) & 1]; PIN2(b2[i], qp[(i) & 1]); \
    if constexpr ((i) & 1) { u32x2 v_ = {qp[0], qp[1]}; *(LAS u32x2*)(size_t)(qw + ((i) >> 1) * 16) = v_; } } while (0)
#define EPI_QA() do { const unsigned LZ_ = GETLANE(); t0 = Lq + (LZ_ >> 2) * 64 + (LZ_ & 3) * 8; \
    t1 = Lq + (16 + ((LZ_ >> 2) & 7)) * 64 + (LZ_ & 3) * 8; PIN2(t0, t1); } while (0)
#define EPI_QR() do { kx[0] = __builtin_bit_cast(u32x2, tr4(t0)); kx[1] = __builtin_bit_cast(u32x2, tr4(t1)); \
    kx[2] = __builtin_bit_cast(u32x2, tr4(t0 + 32)); kx[3] = __builtin_bit_cast(u32x2, tr4(t1 + 32)); } while (0)
#define EPI_Q(z) do { if constexpr ((z) == 0) { u32x4 v_ = {kx[0].x, kx[0].y, kx[2].x, kx[2].y}; BSTI4(v_, va, s_pq, 0); } \
    else { const unsigned LZ_ = GETLANE(); u32x2 v_ = {(LZ_ & 16) ? kx[1].x : kx[1].y, (LZ_ & 16) ? kx[3].x : kx[3].y}; \
      BSTI("dwordx2", v_, (LZ_ & 15) * 16 + ((LZ_ >> 4) & 1) * 8, s_pr, 0); } } while (0)
#define K1C(i) do { a1[i] = isk ? qp[(i) & 1] : 0u; const unsigned r_ = pk(cT1[2 * (i)], cT1[2 * (i) + 1]); b1[i] = is0 ? 0x3F803F80u : r_; PIN2(a1[i], b1[i]); } while (0)
#define KV_MMA(z, s) do { \
    const bf16x8 A_ = mk_frag(a1[4 * (s)], a1[4 * (s) + 1], a1[4 * (s) + 2], a1[4 * (s) + 3]); \
    if constexpr (z) kv2 = mfma32(A_, mk_frag(b2[4 * (s)], b2[4 * (s) + 1], b2[4 * (s) + 2], b2[4 * (s) + 3]), (s) ? kv2 : Z16); \
    else kv1 = mfma32(A_, mk_frag(b1[4 * (s)], b1[4 * (s) + 1], b1[4 * (s) + 2], b1[4 * (s) + 3]), (s) ? kv1 : Z16); } while (0)
#define KV_AD(z) do { const unsigned LZ_ = GETLANE(), c_ = LZ_ & 31, b_ = L0 + SCR + w * 2048 + (LZ_ >> 5) * 16; \
    if constexpr ((z) == 0) { t0 = b_ + (c_ >= 20 ? c_ - 20 : 19 - is0) * 96; PIN(t0); } \
    else if constexpr ((z) == 1) { t1 = b_ + (c_ < 8 ? 12 + __builtin_amdgcn_ubfe(0x54773210u, c_ * 4, 4) : 19) * 96; PIN(t1); } \
    else { t2 = min(12 * w + min(LZ_, 11u), 94u); PIN(t2); } } while (0)
#define KV_WR1(kv, a, i) do { f32x4 v_ = {kv[4 * (i)], kv[4 * (i) + 1], kv[4 * (i) + 2], kv[4 * (i) + 3]}; *(LAS f32x4*)(size_t)((a) + (i) * 32) = v_; } while (0)
#define KV_WR(z) do { if constexpr (z) { KV_WR1(kv2, t1, 0); KV_WR1(kv2, t1, 1); KV_WR1(kv2, t1, 2); } else { KV_WR1(kv1, t0, 0); KV_WR1(kv1, t0, 1); KV_WR1(kv1, t0, 2); } } while (0)
#define KVL(d, i) kvr[d] = *(LAS f32x4*)(size_t)(a_ + (i) * 2048)
#define KV_RD(z) do { const unsigned a_ = L0 + SCR + t2 * 16 + (t2 / 5) * 16; KVL(z, 4 * (z)); KVL((z) + 1, 4 * (z) + 1); KVL((z) + 2, 4 * (z) + 2); KVL((z) + 3, 4 * (z) + 3); } while (0)
#define KV_S(a, b_) kvr[a] = add4(kvr[a], kvr[b_])
#define KV_ST() BST("dwordx4", kvr[0], t2 * 16, (unsigned)((p * 32 + j) * 1536), WS_KVP)
#define PUB(k) do { unsigned z_ = WS_FLAG + (unsigned)((k) * 256 + (p * 32 + j)) * 128; PIN(z_); \
    asm volatile("s_cmp_lg_u32 %3, 0\n\ts_cbranch_scc1 1f\n\ts_mov_b64 exec, 1\n\tbuffer_store_dwordx2 %1, %0, %2, 0 offen sc1\n\ts_mov_b64 exec, -1\n\ts_nop 0\n1:" \
                 ::"v"(z_), "v"(tag), "s"(rws), "s"(w) : "memory", "scc"); } while (0)
#define K1_AFTER_PROLOGUE() do { \
    const int h_ = l >> 5, blk = (l >> 4) & 1, q = (l >> 2) & 3, pp = l & 3; \
    rb[0] = L0 + 32768 + h_ * 2048 + q * 128 + (((q >> 1) & 1) << 6) + blk * 32 + 8 * pp; \
    rb[1] = L0 + 32768 + h_ * 2048 + q * 128 + ((1 ^ ((q >> 1) & 1)) << 6) + blk * 32 + 8 * pp; \
    xr = w * 4096 + (l & 31) * 128 + ((h_ ^ (l & 6)) << 4); \
    asm volatile("" : "+v"(rb[0]), "+v"(rb[1]), "+v"(xr), "+v"(va)); } while (0)
#define SCAL(z) do { \
    if constexpr ((z) == 0) { s_h = WS_H + (unsigned)((16 * p + 2 * w) * 144 + 2 * j) * 1024; s_h1 = s_h + 65536; \
      s_hl = WS_H + (unsigned)(((16 * p + 2 * w) * 144 + 128 + (j >> 1)) * 1024 + (j & 1) * 512); asm volatile("" : "+s"(s_h), "+s"(s_h1), "+s"(s_hl)); } \
    else if constexpr ((z) == 1) { s_pk = WS_PK + (unsigned)(j * 120 + w) * 1024; \
      s_pq = WS_PQM + (unsigned)(((8 * p + w) * 32 + j) * 1024); s_pr = WS_PQR + (unsigned)((j * 64 + 8 * p + w) * 256); asm volatile("" : "+s"(s_pk), "+s"(s_pq), "+s"(s_pr)); } \
    else if constexpr ((z) == 2) { const int a_ = w > 5 ? 5 : w; l_q9a = L0 + (a_ < 3 ? a_ * 9216 : 98304 + (a_ - 3) * 9216); \
      s_q9a = s_pk + (9 * KB(a_) - w) * 1024; asm volatile("" : "+s"(s_q9a), "+s"(l_q9a)); } \
    else if constexpr ((z) == 3) { Lq = L0 + w * 1536 + 27648 + ((w + 5) >> 3) * 93696 + ((w + 2) >> 3) * 28160; asm volatile("" : "+s"(Lq)); const int b_ = 6 + w % 3; l_q9b = L0 + 131072 + (b_ - 6) * 9216; s_q9b = s_pk + (9 * KB(b_) - w) * 1024; asm volatile("" : "+s"(s_q9b), "+s"(l_q9b)); } \
    else if constexpr ((z) == 4) { int q_ = 5 * p + w; const bool d_ = w > 4 || q_ > 35; q_ = d_ ? 0 : q_;        \
      s_lsrc = (unsigned)(16 * q_ * LD_W + (4096 + 16 * j) * 2); s_ldst = WS_PK + (unsigned)(j * 120 + (d_ ? 118 : 81 + q_)) * 1024; asm volatile("" : "+s"(s_lsrc), "+s"(s_ldst)); } \
    else if constexpr ((z) == 5) {   \
      du = 8 * m + w; du = du > 125 ? 125 : du; dq = du / 3; dr = du - 3 * dq; asm volatile("" : "+s"(du), "+s"(dq), "+s"(dr)); } \
    else { const bool o_ = du >= 108; const int t_ = dq >> 1, kx_ = dq & 1, v3_ = dq - 36, ko_ = v3_ >= 3, cg_ = o_ ? v3_ - 3 * ko_ : dr, bl_ = dr; \
      if constexpr ((z) == 6) { const int rw_ = o_ ? 144 * ks + 72 * ko_ + (bl_ < 2 ? 36 * bl_ : 14) \
                           : (t_ < 16 ? 1024 * ks + 128 * (t_ & 7) + 64 * (t_ >> 3) : 4096 + 128 * ks + 64 * (t_ - 16)) + 32 * kx_; \
        s_dsrc = __builtin_amdgcn_readfirstlane(rw_ * LD_X + 288 * n + 96 * cg_); asm volatile("" : "+s"(s_dsrc)); } \
      else if constexpr ((z) == 7) { rdsrc = o_ ? mk_srd(P.wo, 576u * LD_X) : mk_srd(P.wd, 4608u * LD_X); \
        dA = o_ ? (bl_ < 2 ? 2 : 14) : 0; dB = o_ ? (bl_ < 2 ? 4 : 2) : 0; dC = __builtin_amdgcn_readfirstlane((o_ && bl_ == 2) ? 15 : 31); \
        dE = o_ ? 8 : 16; asm volatile("" : "+s"(dA), "+s"(dB), "+s"(dC), "+s"(dE), "+s"(rdsrc)); } \
      else { const int pc_ = o_ ? (cg_ < 2 ? 252 + 18 * cg_ : 360) + 9 * ko_ + 3 * bl_ \
                           : (t_ < 6 ? 18 * t_ + 6 * cg_ : (cg_ < 2 ? 108 + 12 * (t_ - 6) + 6 * cg_ : 288 + 6 * (t_ - 6))) + 3 * kx_; \
        s_ddst = __builtin_amdgcn_readfirstlane(WS_DP + (unsigned)(4 * ks + n) * DP_PITCH + pc_ * 1024); asm volatile("" : "+s"(s_ddst)); } } } while (0)
#define P2_SCAL(z) do { if constexpr ((z) == 0) { \
    rha = mk_srd(P.ws + WS_H + (size_t)((8 * m + 2 * mw + kg) * 144 + kg) * 1024, 144u * 1024); \
    rhb = mk_srd(P.ws + WS_H + (size_t)((8 * m + 2 * mw + (1 - kg)) * 144 + kg) * 1024, 144u * 1024); \
    asm volatile("" : "+s"(rha), "+s"(rhb)); } \
    else if constexpr ((z) == 1) { s_dp = WS_DP + (unsigned)(4 * ks + n) * DP_PITCH + w * 3072; s_slab = WS_SLAB + (unsigned)(4 * m + n) * 16 * 9216; asm volatile("" : "+s"(s_dp), "+s"(s_slab)); } \
    else { s_x1w = L0 + (w < 6 ? 110592 + w * 3072 : 147456 + (w - 6) * 3072); s_x1r = L0 + ((w ^ 4) < 6 ? 110592 + (w ^ 4) * 3072 : 147456 + ((w ^ 4) - 6) * 3072); \
      asm volatile("" : "+s"(s_x1w), "+s"(s_x1r)); } } while (0)
#define K1_TAG() do { PIN2(fl, tw[0]); \
    const u64 e_ = ((u64)(unsigned)__builtin_amdgcn_readfirstlane(fl.y) << 32) | (unsigned)__builtin_amdgcn_readfirstlane(fl.x); \
    tag = e_ * 6364136223846793005ull + 1442695040888963407ull; asm volatile("" : "+s"(tag)); } while (0)
#define T_ADDR(z) do { const unsigned LZ_ = GETLANE(); \
    if constexpr ((z) != 0) {   \
      const unsigned tb_ = (LZ_ & 16) ? ((z) == 2 ? 0x04110F0Eu : 0x08070605u) : ((z) == 2 ? 0x0D0C0B09u : 0x03020100u); \
      ut = L0 + ((LZ_ & 12) + 2 * (LZ_ >> 5)) * 144 + __builtin_amdgcn_ubfe(tb_, (LZ_ & 3) * 8, 8) * 8; PIN(ut); } \
    else { const unsigned c_ = LZ_ & 31; is28 = c_ == 28; is0 = c_ == 0; isk = ((0x000CFFFFu >> c_) & 1) != 0; \
      qw = Lq + (c_ < 8 ? 23 : c_ - 8) * 64 + (LZ_ >> 5) * 8; PIN(qw); } } while (0)
#define L_ADDR() do { ut = L0 + va + LEFT; PIN(ut); } while (0)

#define P2_ADDR1() do { r2[0] = L0 + va + kg * 3072; r2[1] = r2[0] + 49152; r2[2] = r2[0] + 98304; PIN2(r2[0], r2[1]); PIN(r2[2]); } while (0)
#define STR_S(r_, o) DSET(Lw3, o, s_dp, (r_) * 24576)
#define LDA_S(t) do { if constexpr ((t) < 16) asm volatile("s_add_u32 %0, %1, %2" : "=s"(aso) : "s"(ks * 16384), "i"((((t) >> 3) * 64 + 2 * ((t) & 7)) * 1024) : "scc"); \
    else asm volatile("s_add_u32 %0, %1, %2" : "=s"(aso) : "s"(ks * 4096), "i"(131072) : "scc"); } while (0)
#define LDA(t, i) BLDI("dwordx4", A[t][i], va, ((i) ? rhb : rha), aso, ((t) & 1) * 2048)
#define LDF(k, solo) do { const unsigned LZ_ = GETLANE(), i_ = LZ_ & 7; \
    const unsigned row_ = (m >> 1) * 32 + 8 * ks + i_, col_ = i_ * 32 + ((k) == 4 ? 8 * ks + w : j); \
    const unsigned dp_ = i_ * 32 + 4 * (2 * ks + ((LZ_ >> 3) & 1)) + n, par_ = (idx >> 2) * 32 + 4 * (2 * (LZ_ & 3) + (e & 1)) + (idx & 3); \
    const unsigned f_ = (k) == 3 ? ((LZ_ & 16) ? col_ : dp_) : (k) <= 1 ? row_ : ((k) == 4 || (k) == 6) ? col_ \
                      : (k) == 2 ? ((LZ_ & 8) ? col_ : row_) : ((LZ_ & 8) ? par_ : row_); \
    vf = WS_FLAG + ((k) * 256 + f_) * 128; \
    if constexpr (solo) {   fl = __builtin_bit_cast(u32x2, tag); \
      asm volatile("s_cmp_lg_u32 %3, 0\n\ts_cbranch_scc1 1f\n\tbuffer_load_dwordx2 %0, %1, %2, 0 offen sc1\n1:" : "+v"(fl) : "v"(vf), "s"(rws), "s"(w) : "scc"); } \
    else ld_flag(fl, vf, rws); } while (0)
#define CHECKF(k, nn) do { TSB(dv0_); check_flag<nn>(fl, vf, rws, tag); TSB(dv1_); TSB(dv2_); } while (0)
#define RDH(bf, hi, o) fb[bf] = lds_frag(r2[hi] + (o))
#define MMH(bf, c, t, first) do { \
    acc[c][0] = mfma(fb[bf], __builtin_bit_cast(bf16x8, A[t][0]), (first) ? Z : acc[c][0]); \
    acc[c][1] = mfma(fb[bf], __builtin_bit_cast(bf16x8, A[t][1]), (first) ? Z : acc[c][1]); } while (0)
#define DPPADD(a, c) a += __builtin_bit_cast(float, __builtin_amdgcn_update_dpp(0, __builtin_bit_cast(int, a), c, 0xf, 0xf, false))
#define K2_LD() do { const unsigned LZ_ = GETLANE(); unsigned f_ = LZ_ >> 3; f_ = f_ > 5 ? f_ - 2 : f_; t0 = f_; \
    BLD("dword", k2v, (LZ_ & 7) * 49152 + f_ * 4, (unsigned)(j * 1536 + (48 * p + 6 * w) * 4), WS_KVP); } while (0)
#define K2_WAIT(nn) asm volatile("s_waitcnt vmcnt(%1)" : "+v"(k2v) : "i"(nn))
#define K2_S(z) do { if constexpr ((z) == 0) { DPPADD(k2v, 0xB1); DPPADD(k2v, 0x4E); DPPADD(k2v, 0x141); PIN(k2v); } \
    else { const float o_ = __builtin_bit_cast(float, __builtin_amdgcn_update_dpp(0, __builtin_bit_cast(int, k2v), 0x128, 0xf, 0xf, false)); \
      const unsigned v_ = (t0 & 1) ? pk(o_, k2v) : pk(k2v, o_); \
      BST("dword", v_, (t0 >> 1) * 4, (unsigned)(j * 1024 + (48 * p + 6 * w) * 2), WS_KVT); } } while (0)
#define KT_S() DSET(Lw, KVF, (unsigned)((8 * ks + w) * 1024), WS_KVT)
#define RTOF(r_) (8 * m + 2 * mw + ((r_) ? 1 - kg : kg))
#define LDPQ(i) do { if constexpr ((i) == 0) { s_pq = WS_PQM + (unsigned)(((4 * m + mw) * 32 + 8 * ks + 4 * kg) * 1024); \
      s_pr = WS_PQR + (unsigned)(((8 * ks + 4 * kg) * 64 + 4 * m + mw) * 256); asm volatile("" : "+s"(s_pq), "+s"(s_pr)); } \
    if constexpr ((i) < 4) BLDI("dwordx4", pq4[i], va, rws, s_pq, (i) * 1024); \
    else { t2 = (va >> 8) * 16384 + (va & 255); BLDI("dwordx4", pq4[4], t2, rws, s_pr, 0); } } while (0)
#define PQSEL(d, v, a, b_) do { d = kg ? (v).b_ : (v).a; } while (0)
#define WAITPQ(nn) do { \
    asm volatile("s_waitcnt vmcnt(%5)" : "+v"(pq4[0]), "+v"(pq4[1]), "+v"(pq4[2]), "+v"(pq4[3]), "+v"(pq4[4]) : "i"(nn)); \
    gv[0] = __builtin_amdgcn_rcpf(1.0f + __builtin_amdgcn_exp2f(-1.4426950409f * __builtin_bit_cast(float, pq4[4].z << 16))); \
    gv[1] = __builtin_amdgcn_rcpf(1.0f + __builtin_amdgcn_exp2f(-1.4426950409f * __builtin_bit_cast(float, pq4[4].w << 16))); \
    orem[0] = 0u; orem[1] = 0u; gm[0] = 1.0f - (kg ? gv[1] : gv[0]); gm[1] = 1.0f - (kg ? gv[0] : gv[1]); PIN2(gv[0], gv[1]); PIN2(gm[0], gm[1]); } while (0)
#define O_ADDR(z) do { const unsigned LZ_ = GETLANE(); const int lz = LZ_, g_ = lz >> 4, q_ = (lz >> 2) & 3; \
    if constexpr ((z) == 0) { oa[0] = L0 + va + kg * 9216 + 73728; PIN(oa[0]); } \
    else if constexpr ((z) == 1) { vs = (unsigned)((q_ * 4 + ks) * 9216 + (2 * mw + kg) * 1152 + (4 * g_ + (lz & 3)) * 12); PIN(vs); } \
    else { kfa = L0 + KVF + kg * 4096 + (lz & 15) * 40; PIN(kfa); } } while (0)
#define KX(i, z) kx[4 * ((i) & 1) + (z)]
#define O_A(i) do { const unsigned LZ_ = GETLANE(); const int fr_ = LZ_ & 15, g8_ = (LZ_ >> 4) * 8; \
    const unsigned e1_ = L0 + KVF + kg * 4096 + ((fr_ >> 1) == 2 * (i) + 1 ? 16 + (fr_ & 1) : 18) * 40; \
    KX(i, 0) = *(LAS u32x2*)(size_t)(kfa + g8_ + (i) * 1024); KX(i, 1) = *(LAS u32x2*)(size_t)(kfa + 32 + (i) * 1024); \
    KX(i, 2) = *(LAS u32x2*)(size_t)(e1_ + g8_ + (i) * 1024); KX(i, 3) = *(LAS u32x2*)(size_t)(e1_ + 32 + (i) * 1024); } while (0)
#define O_B(i) do { const bool mine_ = (va >> 8) == (i); \
    const bf16x8 f0_ = mk_frag(KX(i, 0).x, KX(i, 0).y, KX(i, 1).x, KX(i, 1).y), f1_ = mk_frag(KX(i, 2).x, KX(i, 2).y, KX(i, 3).x, KX(i, 3).y); \
    const bf16x8 p0_ = mk_frag(pq4[i].x, pq4[i].y, 0u, mine_ ? pq4[4].x : 0u), p1_ = mk_frag(pq4[i].z, pq4[i].w, 0u, mine_ ? pq4[4].y : 0u); \
    n0[(i) & 1][0] = mfma(f0_, p0_, Z); n1[(i) & 1][0] = mfma(f1_, p0_, Z); n0[(i) & 1][1] = mfma(f0_, p1_, Z); n1[(i) & 1][1] = mfma(f1_, p1_, Z); } while (0)
#define O_C(i, rt) do { const bool mine_ = (va >> 8) == (i); \
    const float sc_ = gv[rt] * __builtin_amdgcn_rcpf(n1[(i) & 1][rt][0] + 1e-6f); \
    const f32x4 m_ = mul4(n0[(i) & 1][rt], sc_); om[rt][i][0] = pk(m_[0], m_[1]); om[rt][i][1] = pk(m_[2], m_[3]); \
    float r0_ = n1[(i) & 1][rt][2] * sc_; PIN(r0_); const unsigned rm_ = pk(r0_, n1[(i) & 1][rt][3] * sc_); orem[rt] = mine_ ? rm_ : orem[rt]; \
    asm volatile("" : "+v"(om[rt][i][0]), "+v"(om[rt][i][1]), "+v"(orem[rt])); } while (0)
#define OSW(a, b_) do { const unsigned x_ = a, y_ = b_; a = kg ? y_ : x_; b_ = kg ? x_ : y_; PIN2(a, b_); } while (0)
#define O_SW(i) do { if constexpr ((i) < 4) { OSW(om[0][i][0], om[1][i][0]); OSW(om[0][i][1], om[1][i][1]); } else OSW(orem[0], orem[1]); } while (0)
#define SCALE(c) do { acc[c][0] = mul4(acc[c][0], gm[0]); acc[c][1] = mul4(acc[c][1], gm[1]); } while (0)
#define RDO(bf, o, blk) fb[bf] = lds_frag(oa[0] + ((o) - 73728) + (blk) * 3072)
#define O_SHIFT()
#define OB(rt, blk) ((blk) < 2 ? mk_frag(om[rt][2 * ((blk) & 1)][0], om[rt][2 * ((blk) & 1)][1], om[rt][2 * ((blk) & 1) + 1][0], om[rt][2 * ((blk) & 1) + 1][1]) : mk_frag(0u, orem[rt], 0u, 0u))
#define MMO(bf, c, blk) do { acc[c][0] = mfma(fb[bf], OB(0, blk), acc[c][0]); acc[c][1] = mfma(fb[bf], OB(1, blk), acc[c][1]); } while (0)
#define X_AD(z) do { t0 = ((z) > 0 ? s_x1w : Lw3 + ((z) < 0 ? 110592 : 86016)) + va; t1 = ((z) > 0 ? s_x1r : L0 + (w ^ 4) * 3072 + ((z) < 0 ? 110592 : 86016)) + va; PIN2(t0, t1); } while (0)
#define XW(c) *(LAS f32x4*)(size_t)(t0 + ((c) % 3) * 1024) = acc[c][1]
#define XR(c) xr3[(c) % 3] = *(LAS f32x4*)(size_t)(t1 + ((c) % 3) * 1024)
#define XST(v, o) asm volatile("s_mov_b64 exec, %4\n\tbuffer_store_dwordx3 %0, %1, %2, %3 offen offset:%5 sc1\n\ts_mov_b64 exec, -1\n\ts_nop 0" \
                               ::"v"(v), "v"(vs), "s"(rws), "s"(s_slab), "s"(smask), "i"(o) : "memory")
#define XA(c) do { r[c] = add4(acc[c][0], xr3[(c) % 3]); xp[2 * ((c) % 3)] = pk(r[c][0], r[c][1]); xp[2 * ((c) % 3) + 1] = pk(r[c][2], r[c][3]); \
    PIN2(xp[2 * ((c) % 3)], xp[2 * ((c) % 3) + 1]); } while (0)
#define XS(z) do { u32x3 v_ = {xp[3 * ((z) & 1)], xp[3 * ((z) & 1) + 1], xp[3 * ((z) & 1) + 2]}; XST(v_, (z) * 192); } while (0)
#define XFILL(z) do { if constexpr ((z) == 0) { in[0] = (u32x3){0xFFFFFFFFu, 0xFFFFFFFFu, 0xFFFFFFFFu}; PIN(in[0]); } XST(in[0], (z) * 192); } while (0)
#define EPO_LD() do { const unsigned LZ_ = GETLANE(); eo = WS_FLAG + (unsigned)(w * 32 + (LZ_ & 31)) * 128; ef = __builtin_bit_cast(u32x2, tag); \
    asm volatile("s_cmp_lg_u32 %3, 0\n\ts_cbranch_scc1 1f\n\tbuffer_load_dwordx2 %0, %1, %2, 0 offen sc1\n1:" : "+v"(ef) : "v"(eo), "s"(rws), "s"(b) : "scc"); } while (0)
#define XL_AD() do { const unsigned LZ_ = GETLANE(), q_ = (LZ_ >> 2) & 3; \
    vo = (ks * 4 + (q_ == (unsigned)ks ? (ks ^ 1) : q_)) * 9216 + (2 * mw + kg) * 1152 + (4 * (LZ_ >> 4) + (LZ_ & 3)) * 12; PIN(vo); } while (0)
#define XL(z) BLDI("dwordx3", in[z], vo, rws, s_slab, (z) * 192)

  { unsigned z_ = WS_EPOCH; PIN(z_); ld_flag(fl, z_, rws); }
  STW_S(0, 32768);
  STW(0);
  STW(1);
  STX_S(0, 0);
  STX(0);
  STX(1);
  STX(2);
  STX(3);
  K1_AFTER_PROLOGUE();
  PLAINBAR();
  STW_S(1, 49152);
  STW(0);
  STW(1);
  STX_S(1, 98304);
  STX(0);
  STX(1);
  STX(2);
  STX(3);
  WAITBAR(6, 0);
  K1_TAG();
  RDX(0, 0);
  RDW(0, 0, 0);
  TW_LD(15);
  SCHED();
  RDW(1, 0, 1024);
  TW_LD(16);
  LWAIT(4);
  SCHED();
  RDW(2, 1, 0);
  TW_LD(17);
  SCHED();
  RDW(3, 1, 1024);
  STW_S(2, 65536);
  STW(0);
  STAMP(1);
  LWAIT(4);
  SCHED();
  RDW(4, 0, 4096);
  MMW(0, 0, 0, 0, 0, 1);
  PK_AD(0, 0);
  SCHED();
  RDW(0, 0, 5120);
  MMW(1, 0, 0, 1, 0, 1);
  STW(1);
  LWAIT(4);
  SCHED();
  RDW(1, 1, 4096);
  MMW(2, 0, 1, 0, 0, 1);
  PK_AD(0, 1);
  SCHED();
  RDW(2, 1, 5120);
  MMW(3, 0, 1, 1, 0, 1);
  STX_S(2, 131072);
  STX(0);
  LWAIT(4);
  SCHED();
  RDW(3, 0, 8192);
  MMW(4, 0, 0, 0, 1, 0);
  SCHED();
  RDW(4, 0, 9216);
  MMW(0, 0, 0, 1, 1, 0);
  PK_AD(1, 0);
  LWAIT(4);
  SCHED();
  RDW(0, 1, 8192);
  MMW(1, 0, 1, 0, 1, 0);
  STX(1);
  SCHED();
  RDW(1, 1, 9216);
  MMW(2, 0, 1, 1, 1, 0);
  PK_AD(1, 1);
  LWAIT(4);
  SCHED();
  RDW(2, 0, 12288);
  MMW(3, 0, 0, 0, 2, 0);
  STX(2);
  SCHED();
  RDW(3, 0, 13312);
  MMW(4, 0, 0, 1, 2, 0);
  LWAIT(4);
  SCHED();
  RDW(4, 1, 12288);
  MMW(0, 0, 1, 0, 2, 0);
  STX(3);
  PK_LD(0);
  SCHED();
  RDW(0, 1, 13312);
  MMW(1, 0, 1, 1, 2, 0);
  SCHED();
  WAITBAR(10, 0);
  RDX(1, 98304);
  RDW(1, 0, 16384);
  MMW(2, 0, 0, 0, 3, 0);
  PK_LD(1);
  SCHED();
  RDW(2, 0, 17408);
  MMW(3, 0, 0, 1, 3, 0);
  STW_S(3, 81920);
  STW(0);
  LWAIT(4);
  SCHED();
  RDW(3, 1, 16384);
  MMW(4, 0, 1, 0, 3, 0);
  PK_LD(2);
  SCHED();
  RDW(4, 1, 17408);
  MMW(0, 0, 1, 1, 3, 0);
  STW(1);
  LWAIT(4);
  SCHED();
  RDW(0, 0, 20480);
  MMW(1, 0, 0, 0, 4, 0);
  PK_LD(3);
  SCHED();
  RDW(1, 0, 21504);
  MMW(2, 0, 0, 1, 4, 0);
  LWAIT(4);
  SCHED();
  RDW(2, 1, 20480);
  MMW(3, 0, 1, 0, 4, 0);
  STX_S(3, 0);
  STX(0);
  SCHED();
  RDW(3, 1, 21504);
  MMW(4, 0, 1, 1, 4, 0);
  LWAIT(4);
  SCHED();
  RDW(4, 0, 24576);
  MMW(0, 0, 0, 0, 5, 0);
  STX(1);
  SCHED();
  RDW(0, 0, 25600);
  MMW(1, 0, 0, 1, 5, 0);
  SCAL(0);
  LWAIT(4);
  SCHED();
  RDW(1, 1, 24576);
  MMW(2, 0, 1, 0, 5, 0);
  STX(2);
  SCHED();
  RDW(2, 1, 25600);
  MMW(3, 0, 1, 1, 5, 0);
  LWAIT(4);
  SCHED();
  RDW(3, 0, 28672);
  MMW(4, 0, 0, 0, 6, 0);
  SCHED();
  RDW(4, 0, 29696);
  MMW(0, 0, 0, 1, 6, 0);
  STX(3);
  LWAIT(4);
  SCHED();
  RDW(0, 1, 28672);
  MMW(1, 0, 1, 0, 6, 0);
  SCHED();
  RDW(1, 1, 29696);
  MMW(2, 0, 1, 1, 6, 0);
  STW_S(4, 32768);
  STW(0);
  SCHED();
  WAITBAR(11, 0);
  RDX(2, 131072);
  RDW(2, 0, 32768);
  MMW(3, 0, 0, 0, 7, 0);
  SCAL(1);
  SCHED();
  RDW(3, 0, 33792);
  MMW(4, 0, 0, 1, 7, 0);
  STW(1);
  TW_WAIT(18);
  LWAIT(4);
  SCHED();
  RDW(4, 1, 32768);
  MMW(0, 0, 1, 0, 7, 0);
  TW_LD(25);
  SCHED();
  RDW(0, 1, 33792);
  MMW(1, 0, 1, 1, 7, 0);
  STAMP(2);
  LWAIT(4);
  SCHED();
  RDW(1, 0, 36864);
  MMW(2, 0, 0, 0, 8, 0);
  TW_LD(26);
  STX_S(4, 98304);
  STX(0);
  SCHED();
  RDW(2, 0, 37888);
  MMW(3, 0, 0, 1, 8, 0);
  LWAIT(4);
  SCHED();
  RDW(3, 1, 36864);
  MMW(4, 0, 1, 0, 8, 0);
  TW_LD(27);
  STX(1);
  SCHED();
  RDW(4, 1, 37888);
  MMW(0, 0, 1, 1, 8, 0);
  LWAIT(4);
  SCHED();
  RDW(0, 0, 40960);
  MMW(1, 0, 0, 0, 9, 0);
  SCAL(2);
  SCHED();
  RDW(1, 0, 41984);
  MMW(2, 0, 0, 1, 9, 0);
  STX(2);
  LWAIT(4);
  SCHED();
  RDW(2, 1, 40960);
  MMW(3, 0, 1, 0, 9, 0);
  SCHED();
  RDW(3, 1, 41984);
  MMW(4, 0, 1, 1, 9, 0);
  STX(3);
  LWAIT(4);
  SCHED();
  RDW(4, 0, 45056);
  MMW(0, 0, 0, 0, 10, 0);
  PK_WAIT(13);
  SCHED();
  RDW(0, 0, 46080);
  MMW(1, 0, 0, 1, 10, 0);
  STW_S(5, 49152);
  STW(0);
  LWAIT(4);
  SCHED();
  RDW(1, 1, 45056);
  MMW(2, 0, 1, 0, 10, 0);
  PK_ST(0);
  SCHED();
  RDW(2, 1, 46080);
  MMW(3, 0, 1, 1, 10, 0);
  SCHED();
  WAITBAR(11, 0);
  RDX(3, 0);
  RDW(3, 0, 49152);
  MMW(4, 0, 0, 0, 11, 0);
  STW(1);
  SCHED();
  RDW(4, 0, 50176);
  MMW(0, 0, 0, 1, 11, 0);
  PK_ST(1);
  LWAIT(4);
  SCHED();
  RDW(0, 1, 49152);
  MMW(1, 0, 1, 0, 11, 0);
  STX_S(5, 131072);
  STX(0);
  SCHED();
  RDW(1, 1, 50176);
  MMW(2, 0, 1, 1, 11, 0);
  SCAL(3);
  LWAIT(4);
  SCHED();
  RDW(2, 0, 53248);
  MMW(3, 0, 0, 0, 12, 0);
  SCHED();
  RDW(3, 0, 54272);
  MMW(4, 0, 0, 1, 12, 0);
  STX(1);
  LWAIT(4);
  SCHED();
  RDW(4, 1, 53248);
  MMW(0, 0, 1, 0, 12, 0);
  SCHED();
  RDW(0, 1, 54272);
  MMW(1, 0, 1, 1, 12, 0);
  STX(2);
  LWAIT(4);
  SCHED();
  RDW(1, 0, 57344);
  MMW(2, 0, 0, 0, 13, 0);
  SCHED();
  RDW(2, 0, 58368);
  MMW(3, 0, 0, 1, 13, 0);
  STX(3);
  LWAIT(4);
  SCHED();
  RDW(3, 1, 57344);
  MMW(4, 0, 1, 0, 13, 0);
  SCAL(4);
  SCHED();
  RDW(4, 1, 58368);
  MMW(0, 0, 1, 1, 13, 0);
  LWAIT(4);
  SCHED();
  RDW(0, 0, 61440);
  MMW(1, 0, 0, 0, 14, 0);
  STW_S(6, 65536);
  STW(0);
  SCHED();
  RDW(1, 0, 62464);
  MMW(2, 0, 0, 1, 14, 0);
  LWAIT(4);
  SCHED();
  RDW(2, 1, 61440);
  MMW(3, 0, 1, 0, 14, 0);
  STW(1);
  SCHED();
  RDW(3, 1, 62464);
  MMW(4, 0, 1, 1, 14, 0);
  SCHED();
  WAITBAR(6, 0);
  RDX(4, 98304);
  RDW(4, 0, 0);
  PUB(6);
  MMW(0, 0, 0, 0, 15, 0);
  STX_S(6, 0);
  STX(0);
  SCHED();
  RDW(0, 0, 1024);
  MMW(1, 0, 0, 1, 15, 0);
  LWAIT(4);
  SCHED();
  RDW(1, 1, 0);
  MMW(2, 0, 1, 0, 15, 0);
  SCAL(5);
  SCHED();
  RDW(2, 1, 1024);
  MMW(3, 0, 1, 1, 15, 0);
  STX(1);
  STAMP(3);
  LWAIT(4);
  SCHED();
  RDW(3, 0, 4096);
  MMW(4, 0, 0, 0, 16, 0);
  SCHED();
  RDW(4, 0, 5120);
  MMW(0, 0, 0, 1, 16, 0);
  STX(2);
  LWAIT(4);
  SCHED();
  RDW(0, 1, 4096);
  MMW(1, 0, 1, 0, 16, 0);
  SCHED();
  RDW(1, 1, 5120);
  MMW(2, 0, 1, 1, 16, 0);
  LWAIT(4);
  SCHED();
  RDW(2, 0, 8192);
  MMW(3, 0, 0, 0, 17, 0);
  STX(3);
  SCHED();
  RDW(3, 0, 9216);
  MMW(4, 0, 0, 1, 17, 0);
  LWAIT(4);
  SCHED();
  RDW(4, 1, 8192);
  MMW(0, 0, 1, 0, 17, 0);
  STW_S(7, 81920);
  STW(0);
  SCHED();
  RDW(0, 1, 9216);
  MMW(1, 0, 1, 1, 17, 0);
  SCAL(6);
  LWAIT(4);
  SCHED();
  RDW(1, 0, 12288);
  MMW(2, 0, 0, 0, 18, 0);
  STW(1);
  SCHED();
  RDW(2, 0, 13312);
  MMW(3, 0, 0, 1, 18, 0);
  LWAIT(4);
  SCHED();
  RDW(3, 1, 12288);
  MMW(4, 0, 1, 0, 18, 0);
  SCHED();
  RDW(4, 1, 13312);
  MMW(0, 0, 1, 1, 18, 0);
  STX_S(7, 98304);
  STX(0);
  SCHED();
  WAITBAR(9, 0);
  RDX(5, 131072);
  RDW(0, 0, 16384);
  MMW(1, 0, 0, 0, 19, 0);
  SCHED();
  RDW(1, 0, 17408);
  MMW(2, 0, 0, 1, 19, 0);
  STX(1);
  LWAIT(4);
  SCHED();
  RDW(2, 1, 16384);
  MMW(3, 0, 1, 0, 19, 0);
  TW_WAIT(21);
  SCHED();
  RDW(3, 1, 17408);
  MMW(4, 0, 1, 1, 19, 0);
  STX(2);
  LWAIT(4);
  SCHED();
  RDW(4, 0, 20480);
  MMW(0, 0, 0, 0, 20, 0);
  SCAL(7);
  SCHED();
  RDW(0, 0, 21504);
  MMW(1, 0, 0, 1, 20, 0);
  LWAIT(4);
  SCHED();
  RDW(1, 1, 20480);
  MMW(2, 0, 1, 0, 20, 0);
  STX(3);
  SCHED();
  RDW(2, 1, 21504);
  MMW(3, 0, 1, 1, 20, 0);
  LWAIT(4);
  SCHED();
  RDW(3, 0, 24576);
  MMW(4, 0, 0, 0, 21, 0);
  STW_S(8, 32768);
  STW(0);
  SCHED();
  RDW(4, 0, 25600);
  MMW(0, 0, 0, 1, 21, 0);
  LWAIT(4);
  SCHED();
  RDW(0, 1, 24576);
  MMW(1, 0, 1, 0, 21, 0);
  SCHED();
  RDW(1, 1, 25600);
  MMW(2, 0, 1, 1, 21, 0);
  STW(1);
  LWAIT(4);
  SCHED();
  RDW(2, 0, 28672);
  MMW(3, 0, 0, 0, 22, 0);
  SCAL(8);
  SCHED();
  RDW(3, 0, 29696);
  MMW(4, 0, 0, 1, 22, 0);
  STX_S(8, 131072);
  STX(0);
  LWAIT(4);
  SCHED();
  RDW(4, 1, 28672);
  MMW(0, 0, 1, 0, 22, 0);
  SCHED();
  RDW(0, 1, 29696);
  MMW(1, 0, 1, 1, 22, 0);
  STX(1);
  SCHED();
  WAITBAR(10, 0);
  RDX(6, 0);
  RDW(1, 0, 32768);
  MMW(2, 0, 0, 0, 23, 0);
  LDF(6, 1);
  SCHED();
  RDW(2, 0, 33792);
  MMW(3, 0, 0, 1, 23, 0);
  LWAIT(4);
  SCHED();
  RDW(3, 1, 32768);
  MMW(4, 0, 1, 0, 23, 0);
  STX(2);
  SCHED();
  RDW(4, 1, 33792);
  MMW(0, 0, 1, 1, 23, 0);
  STAMP(4);
  LWAIT(4);
  SCHED();
  RDW(0, 0, 36864);
  MMW(1, 0, 0, 0, 24, 0);
  STX(3);
  SCHED();
  RDW(1, 0, 37888);
  MMW(2, 0, 0, 1, 24, 0);
  LWAIT(4);
  SCHED();
  RDW(2, 1, 36864);
  MMW(3, 0, 1, 0, 24, 0);
  SCHED();
  RDW(3, 1, 37888);
  MMW(4, 0, 1, 1, 24, 0);
  LWAIT(4);
  SCHED();
  RDW(4, 0, 40960);
  MMW(0, 0, 0, 0, 25, 0);
  SCHED();
  RDW(0, 0, 41984);
  MMW(1, 0, 0, 1, 25, 0);
  LWAIT(4);
  SCHED();
  RDW(1, 1, 40960);
  MMW(2, 0, 1, 0, 25, 0);
  SCHED();
  RDW(2, 1, 41984);
  MMW(3, 0, 1, 1, 25, 0);
  LWAIT(4);
  SCHED();
  RDW(3, 0, 45056);
  MMW(4, 0, 0, 0, 26, 0);
  SCHED();
  RDW(4, 0, 46080);
  MMW(0, 0, 0, 1, 26, 0);
  LWAIT(4);
  SCHED();
  RDW(0, 1, 45056);
  MMW(1, 0, 1, 0, 26, 0);
  SCHED();
  RDW(1, 1, 46080);
  MMW(2, 0, 1, 1, 26, 0);
  SCHED();
  CHECKF(6, 2);
  WAITBAR(6, 0);
  RDX(7, 98304);
  RDW(2, 0, 49152);
  MMW(3, 0, 0, 0, 27, 0);
  SCHED();
  RDW(3, 0, 50176);
  MMW(4, 0, 0, 1, 27, 0);
  STQ_S(0, 0);
  STP(0);
  LWAIT(4);
  SCHED();
  RDW(4, 0, 53248);
  MMW(0, 0, 1, 0, 27, 0);
  SCHED();
  RDW(0, 0, 54272);
  MMW(1, 0, 1, 1, 27, 0);
  STQ_S(1, 9216);
  STP(0);
  LWAIT(4);
  SCHED();
  RDW(1, 0, 57344);
  MMW(2, 0, 0, 0, 28, 0);
  SCHED();
  RDW(2, 0, 58368);
  MMW(3, 0, 0, 1, 28, 0);
  STQ_S(2, 18432);
  STP(0);
  LWAIT(4);
  SCHED();
  RDW(3, 0, 61440);
  MMW(4, 0, 0, 0, 29, 0);
  SCHED();
  RDW(4, 0, 62464);
  MMW(0, 0, 0, 1, 29, 0);
  SCHED();
  WAITBAR(3, 0);
  RDX(8, 131072);
  RDW(0, 0, 0);
  MMW(1, 0, 0, 0, 30, 0);
  SCHED();
  RDW(1, 0, 1024);
  MMW(2, 0, 0, 1, 30, 0);
  STQ_S(3, 98304);
  STP(0);
  LWAIT(4);
  SCHED();
  RDW(2, 0, 4096);
  MMW(3, 0, 0, 0, 31, 0);
  SCHED();
  RDW(3, 0, 5120);
  MMW(4, 0, 0, 1, 31, 0);
  STQ_S(4, 107520);
  STP(0);
  LWAIT(4);
  SCHED();
  RDW(4, 0, 8192);
  MMW(0, 0, 0, 0, 32, 0);
  SCHED();
  RDW(0, 0, 9216);
  MMW(1, 0, 0, 1, 32, 0);
  LWAIT(4);
  SCHED();
  RDW(1, 0, 12288);
  MMW(2, 0, 0, 0, 33, 0);
  STQ_S(5, 116736);
  STP(0);
  SCHED();
  RDW(2, 0, 13312);
  MMW(3, 0, 0, 1, 33, 0);
  LWAIT(4);
  SCHED();
  RDW(3, 1, 49152);
  MMW(4, 0, 0, 0, 34, 0);
  SCHED();
  RDW(4, 1, 50176);
  MMW(0, 0, 0, 1, 34, 0);
  H_ADDR();
  STQ9_S(0);
  STP(0);
  LWAIT(4);
  SCHED();
  RDW(0, 1, 53248);
  MMW(1, 0, 0, 0, 35, 0);
  SCHED();
  RDW(1, 1, 54272);
  MMW(2, 0, 0, 1, 35, 0);
  T_ADDR(2);
  STAMP(5);
  LWAIT(4);
  SCHED();
  RDW(2, 1, 57344);
  MMW(3, 0, 1, 0, 28, 0);
  MARK();
  SCHED();
  RDW(3, 1, 58368);
  MMW(4, 0, 1, 1, 28, 0);
  G1(0, 0, 0);
  G1(0, 0, 1);
  G2(0, 0, 0);
  LWAIT(4);
  SCHED();
  RDW(4, 1, 61440);
  MMW(0, 0, 1, 0, 29, 0);
  G2(0, 0, 1);
  G3(0);
  SCHED();
  RDW(0, 1, 62464);
  MMW(1, 0, 1, 1, 29, 0);
  G3(1);
  G4(0);
  LWAIT(4);
  SCHED();
  RDW(1, 1, 0);
  MMW(2, 0, 1, 0, 30, 0);
  G4(1);
  G5(0);
  SCHED();
  RDW(2, 1, 1024);
  MMW(3, 0, 1, 1, 30, 0);
  G5(1);
  G1(0, 0, 2);
  LWAIT(4);
  SCHED();
  RDW(3, 1, 4096);
  MMW(4, 0, 1, 0, 31, 0);
  G1(0, 0, 3);
  G2(0, 0, 2);
  G2(0, 0, 3);
  SCHED();
  RDW(4, 1, 5120);
  MMW(0, 0, 1, 1, 31, 0);
  G3(2);
  G3(3);
  LWAIT(4);
  SCHED();
  RDW(0, 1, 8192);
  MMW(1, 0, 1, 0, 32, 0);
  G4(2);
  G4(3);
  SCHED();
  RDW(1, 1, 9216);
  MMW(2, 0, 1, 1, 32, 0);
  G5(2);
  G5(3);
  LWAIT(4);
  SCHED();
  RDW(2, 1, 12288);
  MMW(3, 0, 1, 0, 33, 0);
  STH(0, 0, 0);
  G1(0, 0, 4);
  G1(0, 0, 5);
  SCHED();
  RDW(3, 1, 13312);
  MMW(4, 0, 1, 1, 33, 0);
  G2(0, 0, 4);
  G2(0, 0, 5);
  SCHED();
  WAITBAR(1, 0);
  RDT(4, 0);
  MMW(0, 0, 1, 0, 34, 0);
  G3(4);
  G3(5);
  SCHED();
  RDT(0, 2304);
  MMW(1, 0, 1, 1, 34, 0);
  STQ_S(6, 131072);
  STP(0);
  G4(4);
  G4(5);
  LWAIT(4);
  SCHED();
  RDT(1, 4608);
  MMW(2, 0, 1, 0, 35, 0);
  G5(4);
  G5(5);
  SCHED();
  RDT(2, 6912);
  MMW(3, 0, 1, 1, 35, 0);
  STQ_S(7, 140288);
  STP(0);
  G1(0, 0, 6);
  G1(0, 0, 7);
  LWAIT(4);
  SCHED();
  RDT(3, 9216);
  MMT2(4, 0, 1);
  G2(0, 0, 6);
  G2(0, 0, 7);
  G3(6);
  SCHED();
  RDT(4, 11520);
  MMT2(0, 1, 0);
  STQ_S(8, 149504);
  STP(0);
  G3(7);
  G4(6);
  LWAIT(4);
  SCHED();
  RDT(0, 13824);
  MMT2(1, 2, 0);
  G4(7);
  G5(6);
  SCHED();
  RDT(1, 16128);
  MMT2(2, 3, 0);
  STQ9_S(1);
  STP(0);
  G5(7);
  STH(0, 0, 1);
  LWAIT(4);
  SCHED();
  RDT(2, 18432);
  MMT2(3, 4, 0);
  G1(0, 1, 0);
  SCHED();
  RDT(3, 20736);
  MMT2(4, 5, 0);
  TW_LD(2);
  G1(0, 1, 1);
  LWAIT(4);
  SCHED();
  RDT(4, 23040);
  MMT2(0, 6, 0);
  G2(0, 1, 0);
  SCHED();
  RDT(0, 25344);
  MMT2(1, 7, 0);
  TW_LD(3);
  G2(0, 1, 1);
  LWAIT(4);
  SCHED();
  SHT(98304);
  RDT(1, 0);
  MMT2(2, 8, 0);
  G3(0);
  SCHED();
  RDT(2, 2304);
  MMT2(3, 9, 0);
  G3(1);
  LWAIT(4);
  SCHED();
  RDT(3, 4608);
  MMT2(4, 10, 0);
  G4(0);
  SCHED();
  RDT(4, 6912);
  MMT2(0, 11, 0);
  G4(1);
  LWAIT(4);
  SCHED();
  RDT(0, 9216);
  MMT2(1, 12, 0);
  G5(0);
  SCHED();
  RDT(1, 11520);
  MMT2(2, 13, 0);
  G5(1);
  G1(0, 1, 2);
  LWAIT(4);
  SCHED();
  RDT(2, 13824);
  MMT2(3, 14, 0);
  LP_AD();
  G1(0, 1, 3);
  SCHED();
  RDT(3, 16128);
  MMT2(4, 15, 0);
  G2(0, 1, 2);
  STAMP(6);
  LWAIT(4);
  SCHED();
  RDT(4, 18432);
  MMT2(0, 16, 0);
  LP_LD(0);
  G2(0, 1, 3);
  SCHED();
  RDT(0, 20736);
  MMT2(1, 17, 0);
  LP_LD(1);
  G3(2);
  G3(3);
  G4(2);
  LWAIT(4);
  SCHED();
  RDT(1, 23040);
  MMT2(2, 18, 0);
  DP_AD(0);
  SCHED();
  RDT(2, 25344);
  MMT2(3, 19, 0);
  G4(3);
  LWAIT(4);
  SCHED();
  WAITBAR(5, 15);
  RDT(3, 32768);
  MMT2(4, 20, 0);
  G5(2);
  SCHED();
  RDT(4, 35072);
  MMT2(0, 21, 0);
  G5(3);
  LWAIT(4);
  SCHED();
  RDT(0, 37376);
  MMT2(1, 22, 0);
  STH(0, 1, 0);
  SCHED();
  RDT(1, 39680);
  MMT2(2, 23, 0);
  DP_LD(0);
  G1(0, 1, 4);
  G1(0, 1, 5);
  G2(0, 1, 4);
  LWAIT(4);
  SCHED();
  RDT(2, 41984);
  MMT2(3, 24, 0);
  DP_AD(1);
  SCHED();
  RDT(3, 44288);
  MMT2(4, 25, 0);
  G2(0, 1, 5);
  LWAIT(4);
  SCHED();
  RDT(4, 46592);
  MMT2(0, 26, 0);
  G3(4);
  SCHED();
  RDT(0, 48896);
  MMT2(1, 27, 0);
  G3(5);
  LWAIT(4);
  SCHED();
  RDT(1, 51200);
  MMT2(2, 28, 0);
  G4(4);
  SCHED();
  RDT(2, 53504);
  MMT2(3, 29, 0);
  DP_LD(1);
  G4(5);
  G5(4);
  LWAIT(4);
  SCHED();
  RDT(3, 55808);
  MMT2(4, 30, 0);
  DP_AD(2);
  G5(5);
  SCHED();
  SHT(49152);
  RDT(4, 8960);
  MMT2(0, 31, 0);
  T_ADDR(1);
  G1(0, 1, 6);
  LWAIT(4);
  SCHED();
  RDT(0, 0);
  MMT2(1, 32, 0);
  G1(0, 1, 7);
  SCHED();
  RDT(1, 2304);
  MMT2(2, 33, 0);
  G2(0, 1, 6);
  LWAIT(4);
  SCHED();
  RDT(2, 4608);
  MMT2(3, 34, 0);
  G2(0, 1, 7);
  SCHED();
  RDT(3, 6912);
  MMT2(4, 35, 0);
  DP_LD(2);
  G3(6);
  LWAIT(4);
  SCHED();
  RDT(4, 9216);
  MMT1(0, 0, 1);
  G3(7);
  SCHED();
  RDT(0, 11520);
  MMT1(1, 1, 0);
  G4(6);
  LWAIT(4);
  SCHED();
  RDT(1, 13824);
  MMT1(2, 2, 0);
  T_ADDR(0);
  G4(7);
  SCHED();
  RDT(2, 16128);
  MMT1(3, 3, 0);
  G5(6);
  G5(7);
  STH(0, 1, 1);
  LWAIT(4);
  SCHED();
  RDT(3, 18432);
  MMT1(4, 4, 0);
  PH1(cT2, 0);
  PH2();
  SCHED();
  RDT(4, 20736);
  MMT1(0, 5, 0);
  PH3(cT2, 0);
  LWAIT(4);
  SCHED();
  RDT(0, 23040);
  MMT1(1, 6, 0);
  STW_S(9, 32768);
  STW(0);
  PH4(0);
  Q3(0);
  SCHED();
  RDT(1, 25344);
  MMT1(2, 7, 0);
  PH1(cT2, 1);
  PH2();
  LWAIT(4);
  SCHED();
  SHT(98304);
  RDT(2, 0);
  MMT1(3, 8, 0);
  STW(1);
  PH3(cT2, 1);
  MARK();
  SCHED();
  RDT(3, 2304);
  MMT1(4, 9, 0);
  PH4(1);
  Q3(1);
  LWAIT(4);
  SCHED();
  RDT(4, 4608);
  MMT1(0, 10, 0);
  PH1(cT2, 2);
  PH2();
  SCHED();
  RDT(0, 6912);
  MMT1(1, 11, 0);
  STW_S(10, 81920);
  STW(0);
  PH3(cT2, 2);
  STAMP(7);
  LWAIT(4);
  SCHED();
  RDT(1, 9216);
  MMT1(2, 12, 0);
  PH4(2);
  Q3(2);
  SCHED();
  RDT(2, 11520);
  MMT1(3, 13, 0);
  STW(1);
  PH1(cT2, 3);
  LWAIT(4);
  SCHED();
  RDT(3, 13824);
  MMT1(4, 14, 0);
  PH2();
  PH3(cT2, 3);
  SCHED();
  RDT(4, 16128);
  MMT1(0, 15, 0);
  PH4(3);
  LWAIT(4);
  SCHED();
  RDT(0, 18432);
  MMT1(1, 16, 0);
  Q3(3);
  PH1(cT2, 4);
  SCHED();
  RDT(1, 20736);
  MMT1(2, 17, 0);
  PH2();
  PH3(cT2, 4);
  LWAIT(4);
  SCHED();
  RDT(2, 23040);
  MMT1(3, 18, 0);
  PH4(4);
  SCHED();
  RDT(3, 25344);
  MMT1(4, 19, 0);
  Q3(4);
  PH1(cT2, 5);
  LWAIT(4);
  SCHED();
  RDT(4, 32768);
  MMT1(0, 20, 0);
  PH2();
  SCHED();
  RDT(0, 35072);
  MMT1(1, 21, 0);
  PH3(cT2, 5);
  PH4(5);
  LWAIT(4);
  SCHED();
  RDT(1, 37376);
  MMT1(2, 22, 0);
  Q3(5);
  PH1(cT2, 6);
  SCHED();
  RDT(2, 39680);
  MMT1(3, 23, 0);
  PH2();
  LWAIT(4);
  SCHED();
  RDT(3, 41984);
  MMT1(4, 24, 0);
  PH3(cT2, 6);
  PH4(6);
  SCHED();
  RDT(4, 44288);
  MMT1(0, 25, 0);
  Q3(6);
  PH1(cT2, 7);
  LWAIT(4);
  SCHED();
  RDT(0, 46592);
  MMT1(1, 26, 0);
  PH2();
  SCHED();
  RDT(1, 48896);
  MMT1(2, 27, 0);
  PH3(cT2, 7);
  LWAIT(4);
  SCHED();
  RDT(2, 51200);
  MMT1(3, 28, 0);
  PH4(7);
  Q3(7);
  SCHED();
  RDT(3, 53504);
  MMT1(4, 29, 0);
  EPI_QA();
  LWAIT(4);
  SCHED();
  RDT(4, 55808);
  MMT1(0, 30, 0);
  SCHED();
  SHT(49152);
  RDT(0, 8960);
  MMT1(1, 31, 0);
  EPI_QR();
  SCHED();
  WAITBAR(0, 0);
  RDW(1, 0, 0);
  PUB(0);
  MMT1(2, 32, 0);
  SCHED();
  RDW(2, 0, 1024);
  MMT1(3, 33, 0);
  STW_S(11, 98304);
  STW(0);
  LWAIT(4);
  SCHED();
  RDW(3, 0, 4096);
  MMT1(4, 34, 0);
  STW(1);
  SCHED();
  RDW(4, 0, 5120);
  MMT1(0, 35, 0);
  STW_S(12, 114688);
  STW(0);
  LWAIT(4);
  SCHED();
  RDW(0, 0, 8192);
  MMW(1, 1, 0, 0, 0, 1);
  STW(1);
  SCHED();
  RDW(1, 0, 9216);
  MMW(2, 1, 0, 1, 0, 1);
  LWAIT(4);
  SCHED();
  RDW(2, 0, 12288);
  MMW(3, 1, 0, 0, 1, 0);
  STW_S(13, 131072);
  STW(0);
  PH1(cT1, 0);
  SCHED();
  RDW(3, 0, 13312);
  MMW(4, 1, 0, 1, 1, 0);
  STW(1);
  PH2();
  LWAIT(4);
  SCHED();
  RDW(4, 0, 49152);
  MMW(0, 1, 0, 0, 2, 0);
  STW_S(14, 0);
  STW(0);
  PH3(cT1, 0);
  SCHED();
  RDW(0, 0, 50176);
  MMW(1, 1, 0, 1, 2, 0);
  PH4(0);
  LWAIT(4);
  SCHED();
  RDW(1, 0, 53248);
  MMW(2, 1, 0, 0, 3, 0);
  STW(1);
  K1C(0);
  DP_WAIT(13);
  SCHED();
  RDW(2, 0, 54272);
  MMW(3, 1, 0, 1, 3, 0);
  STW_S(15, 16384);
  STW(0);
  PH1(cT1, 1);
  PH2();
  TW_WAIT2(20);
  STAMP(8);
  LWAIT(4);
  SCHED();
  SHW0(49152);
  RDW(3, 0, 8192);
  MMW(4, 1, 0, 0, 4, 0);
  STW(1);
  DP_RA(0);
  SCHED();
  RDW(4, 0, 9216);
  MMW(0, 1, 0, 1, 4, 0);
  DP_RD(0);
  PH3(cT1, 1);
  LWAIT(4);
  SCHED();
  RDW(0, 0, 12288);
  MMW(1, 1, 0, 0, 5, 0);
  PH4(1);
  SCHED();
  RDW(1, 0, 13312);
  MMW(2, 1, 0, 1, 5, 0);
  K1C(1);
  LWAIT(4);
  SCHED();
  WAITBAR(6, 15);
  RDW(2, 0, 16384);
  MMW(3, 1, 0, 0, 6, 0);
  DP_ST(0);
  PH1(cT1, 2);
  SCHED();
  RDW(3, 0, 17408);
  MMW(4, 1, 0, 1, 6, 0);
  DP_RD(1);
  PH2();
  LWAIT(4);
  SCHED();
  RDW(4, 0, 20480);
  MMW(0, 1, 0, 0, 7, 0);
  PH3(cT1, 2);
  SCHED();
  RDW(0, 0, 21504);
  MMW(1, 1, 0, 1, 7, 0);
  PH4(2);
  LWAIT(4);
  SCHED();
  RDW(1, 0, 24576);
  MMW(2, 1, 0, 0, 8, 0);
  DP_ST(1);
  K1C(2);
  SCHED();
  RDW(2, 0, 25600);
  MMW(3, 1, 0, 1, 8, 0);
  PH1(cT1, 3);
  LWAIT(4);
  SCHED();
  RDW(3, 0, 28672);
  MMW(4, 1, 0, 0, 9, 0);
  DP_RD(2);
  PH2();
  SCHED();
  RDW(4, 0, 29696);
  MMW(0, 1, 0, 1, 9, 0);
  PH3(cT1, 3);
  LWAIT(4);
  SCHED();
  RDW(0, 0, 32768);
  MMW(1, 1, 0, 0, 10, 0);
  PH4(3);
  SCHED();
  RDW(1, 0, 33792);
  MMW(2, 1, 0, 1, 10, 0);
  DP_ST(2);
  K1C(3);
  PH1(cT1, 4);
  PH2();
  LWAIT(4);
  SCHED();
  RDW(2, 0, 36864);
  MMW(3, 1, 0, 0, 11, 0);
  DP_RA(3);
  PH3(cT1, 4);
  SCHED();
  RDW(3, 0, 37888);
  MMW(4, 1, 0, 1, 11, 0);
  DP_RD(3);
  PH4(4);
  LWAIT(4);
  SCHED();
  RDW(4, 0, 40960);
  MMW(0, 1, 0, 0, 12, 0);
  K1C(4);
  SCHED();
  RDW(0, 0, 41984);
  MMW(1, 1, 0, 1, 12, 0);
  PH1(cT1, 5);
  LWAIT(4);
  SCHED();
  RDW(1, 0, 45056);
  MMW(2, 1, 0, 0, 13, 0);
  DP_ST(3);
  PH2();
  SCHED();
  RDW(2, 0, 46080);
  MMW(3, 1, 0, 1, 13, 0);
  PH3(cT1, 5);
  LWAIT(4);
  SCHED();
  WAITBAR(4, 15);
  RDW(3, 0, 49152);
  MMW(4, 1, 0, 0, 14, 0);
  PH4(5);
  SCHED();
  RDW(4, 0, 50176);
  MMW(0, 1, 0, 1, 14, 0);
  STW_S(16, 49152);
  STW(0);
  K1C(5);
  LWAIT(4);
  SCHED();
  RDW(0, 0, 53248);
  MMW(1, 1, 0, 0, 15, 0);
  STW(1);
  PH1(cT1, 6);
  SCHED();
  RDW(1, 0, 54272);
  MMW(2, 1, 0, 1, 15, 0);
  STW_S(17, 65536);
  STW(0);
  PH2();
  LWAIT(4);
  SCHED();
  SHW0(49152);
  RDW(2, 0, 8192);
  MMW(3, 1, 0, 0, 16, 0);
  STW(1);
  PH3(cT1, 6);
  SCHED();
  RDW(3, 0, 9216);
  MMW(4, 1, 0, 1, 16, 0);
  PH4(6);
  LWAIT(4);
  SCHED();
  RDW(4, 0, 12288);
  MMW(0, 1, 0, 0, 17, 0);
  K1C(6);
  LDF(0, 1);
  SCHED();
  RDW(0, 0, 13312);
  MMW(1, 1, 0, 1, 17, 0);
  PH1(cT1, 7);
  PH2();
  LWAIT(4);
  SCHED();
  SHW0(-131072);
  RDW(1, 0, 0);
  MMW(2, 1, 0, 0, 18, 0);
  EPI_Q(0);
  PH3(cT1, 7);
  SCHED();
  RDW(2, 0, 1024);
  MMW(3, 1, 0, 1, 18, 0);
  PH4(7);
  LWAIT(4);
  SCHED();
  RDW(3, 0, 4096);
  MMW(4, 1, 0, 0, 19, 0);
  K1C(7);
  SCHED();
  RDW(4, 0, 5120);
  MMW(0, 1, 0, 1, 19, 0);
  KV_MMA(0, 0);
  STAMP(9);
  LWAIT(4);
  SCHED();
  RDW(0, 0, 8192);
  MMW(1, 1, 0, 0, 20, 0);
  KV_MMA(0, 1);
  SCHED();
  RDW(1, 0, 9216);
  MMW(2, 1, 0, 1, 20, 0);
  KV_AD(0);
  LWAIT(4);
  SCHED();
  RDW(2, 0, 12288);
  MMW(3, 1, 0, 0, 21, 0);
  EPI_Q(1);
  SCHED();
  RDW(3, 0, 13312);
  MMW(4, 1, 0, 1, 21, 0);
  P2_SCAL(1);
  KV_AD(2);
  LWAIT(4);
  SCHED();
  RDW(4, 0, 16384);
  MMW(0, 1, 0, 0, 22, 0);
  KV_WR(0);
  O_ADDR(1);
  SCHED();
  RDW(0, 0, 17408);
  MMW(1, 1, 0, 1, 22, 0);
  KV_MMA(1, 0);
  LWAIT(4);
  SCHED();
  RDW(1, 0, 20480);
  MMW(2, 1, 0, 0, 23, 0);
  KV_MMA(1, 1);
  XFILL(0);
  SCHED();
  RDW(2, 0, 21504);
  MMW(3, 1, 0, 1, 23, 0);
  KV_AD(1);
  LWAIT(4);
  SCHED();
  RDW(3, 0, 24576);
  MMW(4, 1, 0, 0, 24, 0);
  SCHED();
  RDW(4, 0, 25600);
  MMW(0, 1, 0, 1, 24, 0);
  KV_WR(1);
  LWAIT(4);
  SCHED();
  RDW(0, 0, 28672);
  MMW(1, 1, 0, 0, 25, 0);
  SCHED();
  RDW(1, 0, 29696);
  MMW(2, 1, 0, 1, 25, 0);
  XFILL(1);
  LWAIT(4);
  SCHED();
  WAITBAR(4, 4);
  RDW(2, 0, 49152);
  PUB(3);
  MMW(3, 1, 0, 0, 26, 0);
  KV_RD(0);
  SCHED();
  RDW(3, 0, 50176);
  MMW(4, 1, 0, 1, 26, 0);
  LWAIT(4);
  SCHED();
  RDW(4, 0, 53248);
  MMW(0, 1, 0, 0, 27, 0);
  SCHED();
  RDW(0, 0, 54272);
  MMW(1, 1, 0, 1, 27, 0);
  LWAIT(4);
  SCHED();
  SHW0(49152);
  RDW(1, 0, 8192);
  MMW(2, 1, 0, 0, 28, 0);
  KV_S(0, 1);
  KV_S(2, 3);
  XFILL(2);
  SCHED();
  RDW(2, 0, 9216);
  MMW(3, 1, 0, 1, 28, 0);
  LWAIT(4);
  SCHED();
  RDW(3, 0, 12288);
  MMW(4, 1, 0, 0, 29, 0);
  KV_S(0, 2); KV_RD(1);
  SCHED();
  RDW(4, 0, 13312);
  MMW(0, 1, 0, 1, 29, 0);
  LWAIT(4);
  SCHED();
  RDW(0, 0, 16384);
  MMW(1, 1, 0, 0, 30, 0);
  SCHED();
  RDW(1, 0, 17408);
  MMW(2, 1, 0, 1, 30, 0);
  KV_S(1, 2);
  KV_S(3, 4);
  XFILL(3);
  LWAIT(4);
  SCHED();
  RDW(2, 0, 20480);
  MMW(3, 1, 0, 0, 31, 0);
  KV_S(1, 3);
  SCHED();
  RDW(3, 0, 21504);
  MMW(4, 1, 0, 1, 31, 0);
  LWAIT(4);
  SCHED();
  RDW(4, 0, 24576);
  MMW(0, 1, 0, 0, 32, 0);
  KV_S(0, 1);
  SCHED();
  RDW(0, 0, 25600);
  MMW(1, 1, 0, 1, 32, 0);
  KV_ST();
  LWAIT(4);
  SCHED();
  RDW(1, 0, 28672);
  MMW(2, 1, 0, 0, 33, 0);
  XFILL(4);
  SCHED();
  RDW(2, 0, 29696);
  MMW(3, 1, 0, 1, 33, 0);
  LWAIT(4);
  SCHED();
  RDW(3, 1, 0);
  MMW(4, 1, 0, 0, 34, 0);
  SCHED();
  RDW(4, 1, 1024);
  MMW(0, 1, 0, 1, 34, 0);
  LWAIT(4);
  SCHED();
  RDW(0, 1, 4096);
  MMW(1, 1, 0, 0, 35, 0);
  SCHED();
  RDW(1, 1, 5120);
  MMW(2, 1, 0, 1, 35, 0);
  XFILL(5);
  STAMP(10);
  LWAIT(4);
  SCHED();
  RDW(2, 1, 8192);
  MMW(3, 1, 1, 0, 0, 1);
  SCHED();
  RDW(3, 1, 9216);
  MMW(4, 1, 1, 1, 0, 1);
  LWAIT(4);
  SCHED();
  RDW(4, 1, 12288);
  MMW(0, 1, 1, 0, 1, 0);
  G1(1, 0, 0);
  MARK();
  SCHED();
  RDW(0, 1, 13312);
  MMW(1, 1, 1, 1, 1, 0);
  G1(1, 0, 1);
  LWAIT(4);
  SCHED();
  RDW(1, 1, 49152);
  MMW(2, 1, 1, 0, 2, 0);
  G2(1, 0, 0);
  SCHED();
  RDW(2, 1, 50176);
  MMW(3, 1, 1, 1, 2, 0);
  G2(1, 0, 1);
  LWAIT(4);
  SCHED();
  RDW(3, 1, 53248);
  MMW(4, 1, 1, 0, 3, 0);
  G3(0);
  SCHED();
  RDW(4, 1, 54272);
  MMW(0, 1, 1, 1, 3, 0);
  G3(1);
  LWAIT(4);
  SCHED();
  SHW1(49152);
  RDW(0, 1, 8192);
  MMW(1, 1, 1, 0, 4, 0);
  G4(0);
  SCHED();
  RDW(1, 1, 9216);
  MMW(2, 1, 1, 1, 4, 0);
  G4(1);
  LWAIT(4);
  SCHED();
  RDW(2, 1, 12288);
  MMW(3, 1, 1, 0, 5, 0);
  G5(0);
  SCHED();
  RDW(3, 1, 13312);
  MMW(4, 1, 1, 1, 5, 0);
  LWAIT(4);
  SCHED();
  RDW(4, 1, 16384);
  MMW(0, 1, 1, 0, 6, 0);
  G5(1);
  SCHED();
  RDW(0, 1, 17408);
  MMW(1, 1, 1, 1, 6, 0);
  G1(1, 0, 2);
  LWAIT(4);
  SCHED();
  RDW(1, 1, 20480);
  MMW(2, 1, 1, 0, 7, 0);
  G1(1, 0, 3);
  SCHED();
  RDW(2, 1, 21504);
  MMW(3, 1, 1, 1, 7, 0);
  G2(1, 0, 2);
  LWAIT(4);
  SCHED();
  RDW(3, 1, 24576);
  MMW(4, 1, 1, 0, 8, 0);
  SCHED();
  RDW(4, 1, 25600);
  MMW(0, 1, 1, 1, 8, 0);
  G2(1, 0, 3);
  LWAIT(4);
  SCHED();
  CHECKF(0, 9);
  WAITBAR(2, 15);
  RDW(0, 1, 28672);
  PUB(2);
  MMW(1, 1, 1, 0, 9, 0);
  G3(2);
  SCHED();
  RDW(1, 1, 29696);
  MMW(2, 1, 1, 1, 9, 0);
  G3(3);
  LWAIT(4);
  SCHED();
  RDW(2, 1, 32768);
  MMW(3, 1, 1, 0, 10, 0);
  G4(2);
  SCHED();
  RDW(3, 1, 33792);
  MMW(4, 1, 1, 1, 10, 0);
  G4(3);
  LWAIT(4);
  SCHED();
  RDW(4, 1, 36864);
  MMW(0, 1, 1, 0, 11, 0);
  G5(2);
  LDF(3, 1);
  SCHED();
  RDW(0, 1, 37888);
  MMW(1, 1, 1, 1, 11, 0);
  LWAIT(4);
  SCHED();
  RDW(1, 1, 40960);
  MMW(2, 1, 1, 0, 12, 0);
  G5(3);
  SCHED();
  RDW(2, 1, 41984);
  MMW(3, 1, 1, 1, 12, 0);
  STH(1, 0, 0);
  LWAIT(4);
  SCHED();
  RDW(3, 1, 45056);
  MMW(4, 1, 1, 0, 13, 0);
  G1(1, 0, 4);
  SCHED();
  RDW(4, 1, 46080);
  MMW(0, 1, 1, 1, 13, 0);
  G1(1, 0, 5);
  LWAIT(4);
  SCHED();
  RDW(0, 1, 49152);
  MMW(1, 1, 1, 0, 14, 0);
  G2(1, 0, 4);
  SCHED();
  RDW(1, 1, 50176);
  MMW(2, 1, 1, 1, 14, 0);
  G2(1, 0, 5);
  LWAIT(4);
  SCHED();
  RDW(2, 1, 53248);
  MMW(3, 1, 1, 0, 15, 0);
  G3(4);
  SCHED();
  RDW(3, 1, 54272);
  MMW(4, 1, 1, 1, 15, 0);
  G3(5);
  STAMP(11);
  LWAIT(4);
  SCHED();
  SHW1(49152);
  RDW(4, 1, 8192);
  MMW(0, 1, 1, 0, 16, 0);
  G4(4);
  SCHED();
  RDW(0, 1, 9216);
  MMW(1, 1, 1, 1, 16, 0);
  G4(5);
  LWAIT(4);
  SCHED();
  RDW(1, 1, 12288);
  MMW(2, 1, 1, 0, 17, 0);
  G5(4);
  SCHED();
  RDW(2, 1, 13312);
  MMW(3, 1, 1, 1, 17, 0);
  LWAIT(4);
  SCHED();
  SHW1(-131072);
  RDW(3, 1, 0);
  MMW(4, 1, 1, 0, 18, 0);
  G5(5);
  SCHED();
  RDW(4, 1, 1024);
  MMW(0, 1, 1, 1, 18, 0);
  G1(1, 0, 6);
  LWAIT(4);
  SCHED();
  RDW(0, 1, 4096);
  MMW(1, 1, 1, 0, 19, 0);
  G1(1, 0, 7);
  SCHED();
  RDW(1, 1, 5120);
  MMW(2, 1, 1, 1, 19, 0);
  G2(1, 0, 6);
  LWAIT(4);
  SCHED();
  RDW(2, 1, 8192);
  MMW(3, 1, 1, 0, 20, 0);
  SCHED();
  RDW(3, 1, 9216);
  MMW(4, 1, 1, 1, 20, 0);
  G2(1, 0, 7);
  LWAIT(4);
  SCHED();
  CHECKF(3, 1);
  WAITBAR(63, 15);
  RDW(4, 1, 12288);
  MMW(0, 1, 1, 0, 21, 0);
  G3(6);
  SCHED();
  RDW(0, 1, 13312);
  MMW(1, 1, 1, 1, 21, 0);
  G3(7);
  STL_S(0);
  STP(0);
  LWAIT(4);
  SCHED();
  RDW(1, 1, 16384);
  MMW(2, 1, 1, 0, 22, 0);
  P2_SCAL(0);
  G4(6);
  SCHED();
  RDW(2, 1, 17408);
  MMW(3, 1, 1, 1, 22, 0);
  G4(7);
  LWAIT(4);
  SCHED();
  RDW(3, 1, 20480);
  MMW(4, 1, 1, 0, 23, 0);
  G5(6);
  SCHED();
  RDW(4, 1, 21504);
  MMW(0, 1, 1, 1, 23, 0);
  STP(1);
  STP(2);
  LWAIT(4);
  SCHED();
  RDW(0, 1, 24576);
  MMW(1, 1, 1, 0, 24, 0);
  G5(7);
  SCHED();
  RDW(1, 1, 25600);
  MMW(2, 1, 1, 1, 24, 0);
  STH(1, 0, 1);
  STP(3);
  LWAIT(4);
  SCHED();
  RDW(2, 1, 28672);
  MMW(3, 1, 1, 0, 25, 0);
  LDA_S(0);
  LDA(0, 0);
  SCHED();
  RDW(3, 1, 29696);
  MMW(4, 1, 1, 1, 25, 0);
  STL_S(1);
  STP(0);
  LWAIT(4);
  SCHED();
  RDW(4, 1, 49152);
  MMW(0, 1, 1, 0, 26, 0);
  LDA(0, 1);
  SCHED();
  RDW(0, 1, 50176);
  MMW(1, 1, 1, 1, 26, 0);
  LWAIT(4);
  SCHED();
  RDW(1, 1, 53248);
  MMW(2, 1, 1, 0, 27, 0);
  LDF(2, 1);
  SCHED();
  RDW(2, 1, 54272);
  MMW(3, 1, 1, 1, 27, 0);
  LWAIT(4);
  SCHED();
  SHW1(49152);
  RDW(3, 1, 8192);
  MMW(4, 1, 1, 0, 28, 0);
  SCHED();
  RDW(4, 1, 9216);
  MMW(0, 1, 1, 1, 28, 0);
  LDA(1, 0);
  LWAIT(4);
  SCHED();
  WAITBAR(63, 15);
  RDW(0, 1, 12288);
  MMW(1, 1, 1, 0, 29, 0);
  SCHED();
  RDW(1, 1, 13312);
  MMW(2, 1, 1, 1, 29, 0);
  STR_S(0, 0);
  STP(0);
  LWAIT(4);
  SCHED();
  RDW(2, 1, 16384);
  MMW(3, 1, 1, 0, 30, 0);
  SCHED();
  RDW(3, 1, 17408);
  MMW(4, 1, 1, 1, 30, 0);
  STP(1);
  LWAIT(4);
  SCHED();
  RDW(4, 1, 20480);
  MMW(0, 1, 1, 0, 31, 0);
  LDA(1, 1);
  SCHED();
  RDW(0, 1, 21504);
  MMW(1, 1, 1, 1, 31, 0);
  STAMP(12);
  LWAIT(4);
  SCHED();
  RDW(1, 1, 24576);
  MMW(2, 1, 1, 0, 32, 0);
  L_ADDR();
  STP(2);
  SCHED();
  RDW(2, 1, 25600);
  MMW(3, 1, 1, 1, 32, 0);
  LWAIT(4);
  SCHED();
  RDW(3, 1, 28672);
  MMW(4, 1, 1, 0, 33, 0);
  SCHED();
  RDW(4, 1, 29696);
  MMW(0, 1, 1, 1, 33, 0);
  SCHED();
  CHECKF(2, 5);
  WAITBAR(6, 0);
  RDL(0, 0);
  MMW(1, 1, 1, 0, 34, 0);
  SCHED();
  RDL(1, 1024);
  MMW(2, 1, 1, 1, 34, 0);
  K2_LD();
  STR_S(1, 24576);
  STP(0);
  LWAIT(2);
  SCHED();
  RDL(2, 2048);
  MMW(3, 1, 1, 0, 35, 0);
  STP(1);
  SCHED();
  RDL(3, 3072);
  MMW(4, 1, 1, 1, 35, 0);
  LWAIT(2);
  SCHED();
  RDL(4, 4096);
  MML(0, 0, 1);
  STP(2);
  SCHED();
  RDL(0, 5120);
  MML(1, 1, 0);
  LWAIT(2);
  SCHED();
  RDL(1, 6144);
  MML(2, 2, 0);
  G1(1, 1, 0);
  G1(1, 1, 1);
  SCHED();
  RDL(2, 7168);
  MML(3, 3, 0);
  G2(1, 1, 0);
  STR_S(2, 49152);
  STP(0);
  LWAIT(2);
  SCHED();
  RDL(3, 8192);
  MML(4, 4, 0);
  G2(1, 1, 1);
  STP(1);
  SCHED();
  RDL(4, 9216);
  MML(0, 5, 0);
  G3(0);
  STP(2);
  LWAIT(2);
  SCHED();
  RDL(0, 10240);
  MML(1, 6, 0);
  G3(1);
  SCHED();
  RDL(1, 11264);
  MML(2, 7, 0);
  G4(0);
  STR_S(3, 73728);
  STP(0);
  LWAIT(2);
  SCHED();
  RDL(2, 12288);
  MML(3, 8, 0);
  G4(1);
  STP(1);
  SCHED();
  RDL(3, 13312);
  MML(4, 9, 0);
  G5(0);
  LWAIT(2);
  SCHED();
  RDL(4, 14336);
  MML(0, 10, 0);
  G5(1);
  K2_WAIT(8);
  SCHED();
  RDL(0, 15360);
  MML(1, 11, 0);
  G1(1, 1, 2);
  K2_S(0);
  LWAIT(2);
  SCHED();
  RDL(1, 16384);
  MML(2, 12, 0);
  G1(1, 1, 3);
  STP(2);
  LDA_S(2);
  LDA(2, 0);
  SCHED();
  RDL(2, 17408);
  MML(3, 13, 0);
  G2(1, 1, 2);
  G2(1, 1, 3);
  LWAIT(2);
  SCHED();
  RDL(3, 18432);
  MML(4, 14, 0);
  G3(2);
  K2_S(1);
  SCHED();
  RDL(4, 19456);
  MML(0, 15, 0);
  G3(3);
  LDA(2, 1);
  LWAIT(2);
  SCHED();
  RDL(0, 20480);
  MML(1, 16, 0);
  G4(2);
  LDA(3, 0);
  MARK();
  SCHED();
  RDL(1, 21504);
  MML(2, 17, 0);
  G4(3);
  LDA(3, 1);
  LWAIT(2);
  SCHED();
  RDL(2, 22528);
  MML(3, 18, 0);
  G5(2);
  SCHED();
  RDL(3, 23552);
  MML(4, 19, 0);
  G5(3);
  LWAIT(2);
  SCHED();
  RDL(4, 24576);
  MML(0, 20, 0);
  STH(1, 1, 0);
  LDA_S(4);
  LDA(4, 0);
  LDA(4, 1);
  SCHED();
  RDL(0, 25600);
  MML(1, 21, 0);
  G1(1, 1, 4);
  G1(1, 1, 5);
  LWAIT(2);
  SCHED();
  RDL(1, 26624);
  MML(2, 22, 0);
  G2(1, 1, 4);
  LDA(5, 0);
  SCHED();
  RDL(2, 27648);
  MML(3, 23, 0);
  G2(1, 1, 5);
  LDA(5, 1);
  STAMP(13);
  LWAIT(2);
  SCHED();
  RDL(3, 28672);
  MML(4, 24, 0);
  G3(4);
  SCHED();
  RDL(4, 29696);
  MML(0, 25, 0);
  G3(5);
  LDA_S(6);
  LDA(6, 0);
  LWAIT(2);
  SCHED();
  RDL(0, 30720);
  MML(1, 26, 0);
  G4(4);
  SCHED();
  RDL(1, 31744);
  MML(2, 27, 0);
  G4(5);
  LDA(6, 1);
  LWAIT(2);
  SCHED();
  RDL(2, 32768);
  MML(3, 28, 0);
  G5(4);
  SCHED();
  RDL(3, 33792);
  MML(4, 29, 0);
  G5(5);
  LWAIT(2);
  SCHED();
  RDL(4, 34816);
  MML(0, 30, 0);
  P2_ADDR1();
  G1(1, 1, 6);
  LDA(7, 0);
  SCHED();
  RDL(0, 35840);
  MML(1, 31, 0);
  G1(1, 1, 7);
  LDA(7, 1);
  SCHED();
  WAITBAR(12, 0);
  RDH(1, 0, 0);
  PUB(4);
  MML(2, 32, 0);
  G2(1, 1, 6);
  G2(1, 1, 7);
  SCHED();
  RDH(2, 0, 1024);
  MML(3, 33, 0);
  G3(6);
  STR_S(4, 98304);
  STP(0);
  LWAIT(2);
  SCHED();
  RDH(3, 0, 2048);
  MML(4, 34, 0);
  G3(7);
  SCHED();
  RDH(4, 0, 6144);
  MML(0, 35, 0);
  G4(6);
  STP(1);
  LWAIT(2);
  SCHED();
  RDH(0, 0, 7168);
  WAITA(31, 0);
  MMH(1, 0, 0, 1);
  G4(7);
  SCHED();
  RDH(1, 0, 8192);
  MMH(2, 1, 0, 1);
  G5(6);
  LWAIT(2);
  SCHED();
  RDH(2, 0, 12288);
  MMH(3, 2, 0, 1);
  G5(7);
  SCHED();
  RDH(3, 0, 13312);
  MMH(4, 3, 0, 1);
  STH(1, 1, 1);
  STP(2);
  LWAIT(2);
  SCHED();
  RDH(4, 0, 14336);
  MMH(0, 4, 0, 1);
  GL1(0);
  STR_S(5, 122880);
  STP(0);
  SCHED();
  RDH(0, 0, 18432);
  MMH(1, 5, 0, 1);
  GL2(0);
  LDPQ(0);
  LWAIT(2);
  SCHED();
  RDH(1, 0, 19456);
  MMH(2, 6, 0, 1);
  G3(0);
  STP(1);
  SCHED();
  RDH(2, 0, 20480);
  MMH(3, 7, 0, 1);
  G4(0);
  LWAIT(2);
  SCHED();
  RDH(3, 0, 24576);
  MMH(4, 8, 0, 1);
  G5(0);
  SCHED();
  RDH(4, 0, 25600);
  WAITA(32, 1);
  MMH(0, 0, 1, 0);
  GL1(1);
  STP(2);
  LWAIT(2);
  SCHED();
  RDH(0, 0, 26624);
  MMH(1, 1, 1, 0);
  GL2(1);
  SCHED();
  WAITBAR(5, 15);
  RDH(1, 0, 30720);
  PUB(1);
  MMH(2, 2, 1, 0);
  LDPQ(1);
  LWAIT(2);
  SCHED();
  RDH(2, 0, 31744);
  MMH(3, 3, 1, 0);
  G3(1);
  SCHED();
  RDH(3, 0, 32768);
  MMH(4, 4, 1, 0);
  G4(1);
  LWAIT(2);
  SCHED();
  RDH(4, 0, 36864);
  MMH(0, 5, 1, 0);
  G5(1);
  SCHED();
  RDH(0, 0, 37888);
  MMH(1, 6, 1, 0);
  GL1(2);
  LDPQ(2);
  LWAIT(2);
  SCHED();
  RDH(1, 0, 38912);
  MMH(2, 7, 1, 0);
  GL2(2);
  SCHED();
  RDH(2, 0, 43008);
  MMH(3, 8, 1, 0);
  G3(2);
  STAMP(14);
  LWAIT(2);
  SCHED();
  RDH(3, 0, 44032);
  WAITA(21, 2);
  MMH(4, 0, 2, 0);
  SCHED();
  RDH(4, 0, 45056);
  MMH(0, 1, 2, 0);
  G4(2);
  LDPQ(3);
  LWAIT(2);
  SCHED();
  RDH(0, 1, 0);
  MMH(1, 2, 2, 0);
  LDF(4, 0);
  SCHED();
  RDH(1, 1, 1024);
  MMH(2, 3, 2, 0);
  G5(2);
  LWAIT(2);
  SCHED();
  RDH(2, 1, 2048);
  MMH(3, 4, 2, 0);
  GL1(3);
  SCHED();
  RDH(3, 1, 6144);
  MMH(4, 5, 2, 0);
  GL2(3);
  LWAIT(2);
  SCHED();
  RDH(4, 1, 7168);
  MMH(0, 6, 2, 0);
  G3(3);
  LDPQ(4);
  SCHED();
  RDH(0, 1, 8192);
  MMH(1, 7, 2, 0);
  G4(3);
  LWAIT(2);
  SCHED();
  RDH(1, 1, 12288);
  MMH(2, 8, 2, 0);
  G5(3);
  SCHED();
  RDH(2, 1, 13312);
  WAITA(22, 3);
  MMH(3, 0, 3, 0);
  ST_L();
  LWAIT(2);
  SCHED();
  RDH(3, 1, 14336);
  MMH(4, 1, 3, 0);
  SCHED();
  RDH(4, 1, 18432);
  MMH(0, 2, 3, 0);
  LWAIT(2);
  SCHED();
  RDH(0, 1, 19456);
  MMH(1, 3, 3, 0);
  SCHED();
  CHECKF(4, 2);
  RDH(1, 1, 20480);
  MMH(2, 4, 3, 0);
  LWAIT(2);
  SCHED();
  WAITBAR(0, 15);
  RDH(2, 1, 24576);
  PUB(5);
  MMH(3, 5, 3, 0);
  KT_S();
  STP(0);
  SCHED();
  RDH(3, 1, 25600);
  MMH(4, 6, 3, 0);
  LDF(1, 0);
  LWAIT(2);
  SCHED();
  RDH(4, 1, 26624);
  MMH(0, 7, 3, 0);
  STR_S(6, 0);
  STP(0);
  SCHED();
  RDH(0, 1, 30720);
  MMH(1, 8, 3, 0);
  STP(1);
  STAMP(15);
  LWAIT(2);
  SCHED();
  RDH(1, 1, 31744);
  WAITA(24, 4);
  MMH(2, 0, 4, 0);
  SCHED();
  RDH(2, 1, 32768);
  MMH(3, 1, 4, 0);
  STP(2);
  LWAIT(2);
  SCHED();
  RDH(3, 1, 36864);
  MMH(4, 2, 4, 0);
  SCHED();
  RDH(4, 1, 37888);
  MMH(0, 3, 4, 0);
  STR_S(7, 24576);
  STP(0);
  LWAIT(2);
  SCHED();
  RDH(0, 1, 38912);
  MMH(1, 4, 4, 0);
  SCHED();
  RDH(1, 1, 43008);
  MMH(2, 5, 4, 0);
  LWAIT(2);
  SCHED();
  RDH(2, 1, 44032);
  MMH(3, 6, 4, 0);
  SCHED();
  RDH(3, 1, 45056);
  MMH(4, 7, 4, 0);
  LWAIT(2);
  SCHED();
  RDH(4, 2, 0);
  MMH(0, 8, 4, 0);
  SCHED();
  CHECKF(1, 4);
  RDH(0, 2, 1024);
  WAITA(24, 5);
  MMH(1, 0, 5, 0);
  STP(1);
  LWAIT(2);
  SCHED();
  RDH(1, 2, 2048);
  MMH(2, 1, 5, 0);
  LDA_S(8);
  LDA(8, 0);
  SCHED();
  RDH(2, 2, 6144);
  MMH(3, 2, 5, 0);
  LWAIT(2);
  SCHED();
  RDH(3, 2, 7168);
  MMH(4, 3, 5, 0);
  LDA(8, 1);
  SCHED();
  RDH(4, 2, 8192);
  MMH(0, 4, 5, 0);
  LWAIT(2);
  SCHED();
  RDH(0, 2, 12288);
  MMH(1, 5, 5, 0);
  SCHED();
  RDH(1, 2, 13312);
  MMH(2, 6, 5, 0);
  LDA(9, 0);
  LWAIT(2);
  SCHED();
  RDH(2, 2, 14336);
  MMH(3, 7, 5, 0);
  STP(2);
  SCHED();
  RDH(3, 2, 18432);
  MMH(4, 8, 5, 0);
  STAMP(16);
  LWAIT(2);
  SCHED();
  RDH(4, 2, 19456);
  WAITA(27, 6);
  MMH(0, 0, 6, 0);
  O_ADDR(2);
  LDA(9, 1);
  SCHED();
  RDH(0, 2, 20480);
  MMH(1, 1, 6, 0);
  LWAIT(2);
  SCHED();
  RDH(1, 2, 24576);
  MMH(2, 2, 6, 0);
  LDA_S(10);
  LDA(10, 0);
  SCHED();
  RDH(2, 2, 25600);
  MMH(3, 3, 6, 0);
  LWAIT(2);
  SCHED();
  RDH(3, 2, 26624);
  MMH(4, 4, 6, 0);
  SCHED();
  WAITBAR(8, 15);
  RDH(4, 2, 30720);
  MMH(0, 5, 6, 0);
  LDA(10, 1);
  LWAIT(2);
  SCHED();
  RDH(0, 2, 31744);
  WAITA(28, 7);
  MMH(1, 0, 7, 0);
  STR_S(8, 49152);
  STP(0);
  WAITPQ(16);
  SCHED();
  RDH(1, 2, 32768);
  MMH(2, 1, 7, 0);
  O_A(0);
  LWAIT(2);
  SCHED();
  RDH(2, 2, 36864);
  MMH(3, 2, 7, 0);
  STP(1);
  LDA(11, 0);
  SCHED();
  RDH(3, 2, 37888);
  MMH(4, 3, 7, 0);
  LWAIT(2);
  SCHED();
  RDH(4, 2, 38912);
  MMH(0, 4, 7, 0);
  STP(2);
  O_A(1);
  SCHED();
  RDH(0, 2, 43008);
  MMH(1, 5, 7, 0);
  LDA(11, 1);
  LWAIT(2);
  SCHED();
  RDH(1, 2, 44032);
  WAITA(10, 8);
  MMH(2, 0, 8, 0);
  LDF(5, 1);
  SCHED();
  RDH(2, 2, 45056);
  MMH(3, 1, 8, 0);
  STR_S(9, 73728);
  STP(0);
  LWAIT(2);
  SCHED();
  RDH(3, 0, 0);
  MMH(4, 2, 8, 0);
  LDA_S(12);
  LDA(12, 0);
  SCHED();
  RDH(4, 0, 1024);
  MMH(0, 3, 8, 0);
  O_B(0);
  LWAIT(2);
  SCHED();
  RDH(0, 0, 2048);
  MMH(1, 4, 8, 0);
  STP(1);
  LDA(12, 1);
  SCHED();
  RDH(1, 0, 6144);
  MMH(2, 5, 8, 0);
  O_A(2);
  STAMP(17);
  LWAIT(2);
  SCHED();
  RDH(2, 0, 7168);
  WAITA(11, 9);
  MMH(3, 0, 9, 0);
  MARK();
  SCHED();
  RDH(3, 0, 8192);
  MMH(4, 1, 9, 0);
  LDA(13, 0);
  LWAIT(2);
  SCHED();
  RDH(4, 0, 12288);
  MMH(0, 2, 9, 0);
  O_C(0, 0);
  SCHED();
  RDH(0, 0, 13312);
  MMH(1, 3, 9, 0);
  STP(2);
  LWAIT(2);
  SCHED();
  RDH(1, 0, 14336);
  MMH(2, 4, 9, 0);
  LDA(13, 1);
  SCHED();
  RDH(2, 0, 18432);
  MMH(3, 5, 9, 0);
  O_C(0, 1);
  LWAIT(2);
  SCHED();
  RDH(3, 0, 19456);
  WAITA(12, 10);
  MMH(4, 0, 10, 0);
  SCHED();
  RDH(4, 0, 20480);
  MMH(0, 1, 10, 0);
  LDA_S(14);
  LDA(14, 0);
  LWAIT(2);
  SCHED();
  CHECKF(5, 8);
  WAITBAR(9, 15);
  RDH(0, 0, 24576);
  MMH(1, 2, 10, 0);
  O_SW(0);
  SCHED();
  RDH(1, 0, 25600);
  MMH(2, 3, 10, 0);
  STR_S(10, 98304);
  STP(0);
  LDA(14, 1);
  LWAIT(2);
  SCHED();
  RDH(2, 0, 26624);
  MMH(3, 4, 10, 0);
  O_B(1);
  SCHED();
  RDH(3, 0, 30720);
  MMH(4, 5, 10, 0);
  STP(1);
  LWAIT(2);
  SCHED();
  RDH(4, 0, 31744);
  WAITA(11, 11);
  MMH(0, 0, 11, 0);
  LDA(15, 0);
  O_A(3);
  SCHED();
  RDH(0, 0, 32768);
  MMH(1, 1, 11, 0);
  STP(2);
  LWAIT(2);
  SCHED();
  RDH(1, 0, 36864);
  MMH(2, 2, 11, 0);
  SCHED();
  RDH(2, 0, 37888);
  MMH(3, 3, 11, 0);
  STR_S(11, 122880);
  STP(0);
  LDA(15, 1);
  LWAIT(2);
  SCHED();
  RDH(3, 0, 38912);
  MMH(4, 4, 11, 0);
  O_C(1, 0);
  SCHED();
  RDH(4, 0, 43008);
  MMH(0, 5, 11, 0);
  STAMP(18);
  LWAIT(2);
  SCHED();
  RDH(0, 0, 44032);
  WAITA(11, 12);
  MMH(1, 0, 12, 0);
  LDA_S(16);
  LDA(16, 0);
  SCHED();
  RDH(1, 0, 45056);
  MMH(2, 1, 12, 0);
  O_C(1, 1);
  LWAIT(2);
  SCHED();
  RDH(2, 1, 0);
  MMH(3, 2, 12, 0);
  STP(1);
  SCHED();
  RDH(3, 1, 1024);
  MMH(4, 3, 12, 0);
  LDA(16, 1);
  LWAIT(2);
  SCHED();
  RDH(4, 1, 2048);
  MMH(0, 4, 12, 0);
  O_SW(1);
  SCHED();
  RDH(0, 1, 6144);
  MMH(1, 5, 12, 0);
  O_B(2);
  LWAIT(2);
  SCHED();
  RDH(1, 1, 7168);
  WAITA(11, 13);
  MMH(2, 0, 13, 0);
  LDA(17, 0);
  MARK();
  SCHED();
  RDH(2, 1, 8192);
  MMH(3, 1, 13, 0);
  LWAIT(2);
  SCHED();
  RDH(3, 1, 12288);
  MMH(4, 2, 13, 0);
  STP(2);
  SCHED();
  RDH(4, 1, 13312);
  MMH(0, 3, 13, 0);
  LDA(17, 1);
  LWAIT(2);
  SCHED();
  RDH(0, 1, 14336);
  MMH(1, 4, 13, 0);
  O_C(2, 0);
  SCHED();
  RDH(1, 1, 18432);
  MMH(2, 5, 13, 0);
  O_C(2, 1);
  LWAIT(2);
  SCHED();
  RDH(2, 1, 19456);
  WAITA(11, 14);
  MMH(3, 0, 14, 0);
  P2_SCAL(2);
  SCHED();
  RDH(3, 1, 20480);
  MMH(4, 1, 14, 0);
  LWAIT(2);
  SCHED();
  WAITBAR(8, 15);
  RDH(4, 1, 24576);
  MMH(0, 2, 14, 0);
  SCHED();
  RDH(0, 1, 25600);
  MMH(1, 3, 14, 0);
  STR_S(12, 0);
  STP(0);
  O_SW(2);
  LWAIT(2);
  SCHED();
  RDH(1, 1, 26624);
  MMH(2, 4, 14, 0);
  O_B(3);
  SCHED();
  RDH(2, 1, 30720);
  MMH(3, 5, 14, 0);
  STP(1);
  STAMP(19);
  LWAIT(2);
  SCHED();
  RDH(3, 1, 31744);
  WAITA(8, 15);
  MMH(4, 0, 15, 0);
  SCHED();
  RDH(4, 1, 32768);
  MMH(0, 1, 15, 0);
  LWAIT(2);
  SCHED();
  RDH(0, 1, 36864);
  MMH(1, 2, 15, 0);
  STP(2);
  SCHED();
  RDH(1, 1, 37888);
  MMH(2, 3, 15, 0);
  O_C(3, 0);
  LWAIT(2);
  SCHED();
  RDH(2, 1, 38912);
  MMH(3, 4, 15, 0);
  O_C(3, 1);
  SCHED();
  RDH(3, 1, 43008);
  MMH(4, 5, 15, 0);
  STR_S(13, 24576);
  STP(0);
  LWAIT(2);
  SCHED();
  RDH(4, 1, 44032);
  WAITA(7, 16);
  MMH(0, 0, 16, 0);
  O_ADDR(0);
  SCHED();
  RDH(0, 1, 45056);
  MMH(1, 1, 16, 0);
  LWAIT(2);
  SCHED();
  RDH(1, 2, 0);
  MMH(2, 2, 16, 0);
  STP(1);
  O_SW(3);
  SCHED();
  RDH(2, 2, 1024);
  MMH(3, 3, 16, 0);
  O_SW(4);
  LWAIT(2);
  SCHED();
  RDH(3, 2, 2048);
  MMH(4, 4, 16, 0);
  SCHED();
  RDH(4, 2, 6144);
  MMH(0, 5, 16, 0);
  STP(2);
  LWAIT(2);
  SCHED();
  RDH(0, 2, 7168);
  WAITA(6, 17);
  MMH(1, 0, 17, 0);
  SCHED();
  RDH(1, 2, 8192);
  MMH(2, 1, 17, 0);
  SCHED();
  WAITBAR(7, 0);
  RDO(2, 110592, 0);
  MMH(3, 2, 17, 0);
  SCALE(0);
  SCHED();
  RDO(3, 111616, 0);
  MMH(4, 3, 17, 0);
  STR_S(14, 49152);
  STP(0);
  SCALE(1);
  LWAIT(2);
  SCHED();
  RDO(4, 112640, 0);
  MMH(0, 4, 17, 0);
  SCALE(2);
  SCHED();
  RDO(0, 110592, 1);
  MMH(1, 5, 17, 0);
  SCALE(3);
  STAMP(20);
  LWAIT(2);
  SCHED();
  RDO(1, 111616, 1);
  MMO(2, 0, 0);
  SCALE(4);
  SCHED();
  RDO(2, 112640, 1);
  MMO(3, 1, 0);
  SCALE(5);
  LWAIT(2);
  SCHED();
  RDO(3, 110592, 2);
  MMO(4, 2, 0);
  STP(1);
  SCHED();
  RDO(4, 111616, 2);
  MMO(0, 0, 1);
  LWAIT(2);
  SCHED();
  RDO(0, 112640, 2);
  MMO(1, 1, 1);
  SCHED();
  RDO(1, 129024, 0);
  MMO(2, 2, 1);
  LWAIT(2);
  SCHED();
  RDO(2, 130048, 0);
  MMO(3, 0, 2);
  SCHED();
  RDO(3, 131072, 0);
  MMO(4, 1, 2);
  X_AD(0);
  LWAIT(2);
  SCHED();
  RDO(4, 129024, 1);
  MMO(0, 2, 2);
  STP(2);
  SCHED();
  RDO(0, 130048, 1);
  MMO(1, 3, 0);
  XW(0);
  LWAIT(2);
  SCHED();
  RDO(1, 131072, 1);
  MMO(2, 4, 0);
  XW(1);
  SCHED();
  RDO(2, 129024, 2);
  MMO(3, 5, 0);
  XW(2);
  LWAIT(2);
  SCHED();
  RDO(3, 130048, 2);
  MMO(4, 3, 1);
  SCHED();
  RDO(4, 131072, 2);
  MMO(0, 4, 1);
  LWAIT(2);
  SCHED();
  WAITBAR(6, 4);
  RDH(0, 0, 0);
  MMO(1, 5, 1);
  XR(0);
  SCHED();
  RDH(1, 0, 1024);
  MMO(2, 3, 2);
  XR(1);
  LWAIT(2);
  SCHED();
  RDH(2, 0, 2048);
  MMO(3, 4, 2);
  XR(2);
  SCHED();
  RDH(3, 0, 6144);
  MMO(4, 5, 2);
  X_AD(1);
  STAMP(21);
  LWAIT(2);
  SCHED();
  RDH(4, 0, 7168);
  MMH(0, 6, 6, 0);
  XA(0);
  MARK();
  SCHED();
  RDH(0, 0, 8192);
  MMH(1, 7, 6, 0);
  XW(3);
  XA(1);
  LWAIT(2);
  SCHED();
  RDH(1, 0, 12288);
  MMH(2, 8, 6, 0);
  XW(4);
  XA(2);
  SCHED();
  RDH(2, 0, 13312);
  MMH(3, 6, 7, 0);
  XW(5);
  XS(0);
  LWAIT(2);
  SCHED();
  RDH(3, 0, 14336);
  MMH(4, 7, 7, 0);
  XS(1);
  SCHED();
  RDH(4, 0, 18432);
  MMH(0, 8, 7, 0);
  LWAIT(2);
  SCHED();
  WAITBAR(2, 4);
  RDH(0, 0, 19456);
  MMH(1, 6, 8, 0);
  XR(3);
  SCHED();
  RDH(1, 0, 20480);
  MMH(2, 7, 8, 0);
  STR_S(15, 73728);
  STP(0);
  XR(4);
  LWAIT(2);
  SCHED();
  RDH(2, 0, 24576);
  MMH(3, 8, 8, 0);
  XR(5);
  SCHED();
  RDH(3, 0, 25600);
  MMH(4, 6, 9, 0);
  STP(1);
  LWAIT(2);
  SCHED();
  RDH(4, 0, 26624);
  MMH(0, 7, 9, 0);
  XA(3);
  SCHED();
  RDH(0, 0, 30720);
  MMH(1, 8, 9, 0);
  STP(2);
  XA(4);
  LWAIT(2);
  SCHED();
  RDH(1, 0, 31744);
  MMH(2, 6, 10, 0);
  XA(5);
  SCHED();
  RDH(2, 0, 32768);
  MMH(3, 7, 10, 0);
  XS(2);
  LWAIT(2);
  SCHED();
  RDH(3, 0, 36864);
  MMH(4, 8, 10, 0);
  XS(3);
  SCHED();
  RDH(4, 0, 37888);
  MMH(0, 6, 11, 0);
  LWAIT(2);
  SCHED();
  RDH(0, 0, 38912);
  MMH(1, 7, 11, 0);
  SCHED();
  RDH(1, 0, 43008);
  MMH(2, 8, 11, 0);
  STAMP(22);
  LWAIT(2);
  SCHED();
  RDH(2, 0, 44032);
  MMH(3, 6, 12, 0);
  SCHED();
  RDH(3, 0, 45056);
  MMH(4, 7, 12, 0);
  LWAIT(2);
  SCHED();
  RDH(4, 1, 0);
  MMH(0, 8, 12, 0);
  O_SHIFT();
  SCHED();
  RDH(0, 1, 1024);
  MMH(1, 6, 13, 0);
  LWAIT(2);
  SCHED();
  RDH(1, 1, 2048);
  MMH(2, 7, 13, 0);
  SCHED();
  RDH(2, 1, 6144);
  MMH(3, 8, 13, 0);
  LWAIT(2);
  SCHED();
  RDH(3, 1, 7168);
  MMH(4, 6, 14, 0);
  EPO_LD();
  SCHED();
  RDH(4, 1, 8192);
  MMH(0, 7, 14, 0);
  LWAIT(2);
  SCHED();
  RDH(0, 1, 12288);
  MMH(1, 8, 14, 0);
  SCHED();
  RDH(1, 1, 13312);
  MMH(2, 6, 15, 0);
  LWAIT(2);
  SCHED();
  RDH(2, 1, 14336);
  MMH(3, 7, 15, 0);
  SCHED();
  RDH(3, 1, 18432);
  MMH(4, 8, 15, 0);
  XL_AD();
  LWAIT(2);
  SCHED();
  RDH(4, 1, 19456);
  MMH(0, 6, 16, 0);
  SCHED();
  RDH(0, 1, 20480);
  MMH(1, 7, 16, 0);
  LWAIT(2);
  SCHED();
  WAITBAR(2, 15);
  RDO(1, 73728, 0);
  MMH(2, 8, 16, 0);
  SCHED();
  RDO(2, 74752, 0);
  MMH(3, 6, 17, 0);
  XL(0);
  LWAIT(2);
  SCHED();
  RDO(3, 75776, 0);
  MMH(4, 7, 17, 0);
  SCHED();
  RDO(4, 73728, 1);
  MMH(0, 8, 17, 0);
  XL(1);
  SCALE(6);
  STAMP(23);
  LWAIT(2);
  SCHED();
  RDO(0, 74752, 1);
  MMO(1, 6, 0);
  SCALE(7);
  SCHED();
  RDO(1, 75776, 1);
  MMO(2, 7, 0);
  SCALE(8);
  LWAIT(2);
  SCHED();
  RDO(2, 73728, 2);
  MMO(3, 8, 0);
  XL(2);
  SCHED();
  RDO(3, 74752, 2);
  MMO(4, 6, 1);
  LWAIT(2);
  SCHED();
  RDO(4, 75776, 2);
  MMO(0, 7, 1);
  XL(3);
  SCHED();
  MMO(1, 8, 1);
  SCHED();
  MMO(2, 6, 2);
  SCHED();
  MMO(3, 7, 2);
  SCHED();
  MMO(4, 8, 2);
  SCHED();

  X_AD(-1); XW(6); XW(7); XW(8);
  {
    const int l = GETLANE(), g = l >> 4, q = (l >> 2) & 3, pp = l & 3;
    const int rtl = 2 * mw + kg;
    check_flag<4>(ef, eo, rws, tag);
    wait_lds_bar();
    STAMP(24);
    XR(6); XR(7); XR(8);
    XA(6); XA(7); XS(4); XA(8); XS(5);
    XL(4); XL(5);
    if (b == 0 && w == 7 && l == 0) { unsigned z = WS_EPOCH; PIN(z); BSTI("dwordx2", tag, z, 0u, 0); }
    STAMP(25);
#define UMAX3(c) max(max(in[c].x, in[c].y), in[c].z)
#define XPOLL(a, b_) for (int spin = 0; spin < 200000; ++spin) { \
      if (!__any(max(UMAX3(a), UMAX3(b_)) == 0xFFFFFFFFu)) break; \
      XL(a); XL(b_); \
      asm volatile("s_waitcnt vmcnt(0)" : "+v"(in[a]), "+v"(in[b_]) :: "memory"); }
    asm volatile("s_waitcnt vmcnt(4)" : "+v"(in[0]), "+v"(in[1]), "+v"(in[2]), "+v"(in[3]) :: "memory");
    XPOLL(0, 1)
    XPOLL(2, 3)
    u32x2 ov[9];
#define XD(c, i) in[2 * ((c) / 3) + (2 * ((c) % 3) + (i)) / 3][(2 * ((c) % 3) + (i)) % 3]
#define XLO(u) __builtin_bit_cast(float, (u) << 16)
#define XHI(u) __builtin_bit_cast(float, (u) & 0xffff0000u)
#define XFIN(c) do { const unsigned d0_ = XD(c, 0), d1_ = XD(c, 1); const bool own_ = q == ks; \
      ov[c].x = pk(sum4(own_ ? r[c][0] : XLO(d0_)), sum4(own_ ? r[c][1] : XHI(d0_))); ov[c].y = pk(sum4(own_ ? r[c][2] : XLO(d1_)), sum4(own_ ? r[c][3] : XHI(d1_))); } while (0)
    XFIN(0); XFIN(1); XFIN(2); XFIN(3); XFIN(4); XFIN(5);
    char* op = P.out + (size_t)(128 * m + 16 * rtl + 4 * ks + pp) * LD_X + (144 * n + 4 * g) * 2;
    {
      const u32x2 a = q == 0 ? ov[0] : (q == 1 ? ov[1] : (q == 2 ? ov[2] : ov[3]));
      asm volatile("global_store_dwordx2 %0, %1, off\n\ts_nop 1" ::"v"(op + q * 32), "v"(a) : "memory");
      const u32x2 c = (q & 1) ? ov[5] : ov[4];
      asm volatile("global_store_dwordx2 %0, %1, off\n\ts_nop 1" ::"v"(op + (4 + (q & 1)) * 32), "v"(c) : "memory");
    }
    STAMP(26);
    asm volatile("s_waitcnt vmcnt(2)" : "+v"(in[4]), "+v"(in[5]) :: "memory");
    XPOLL(4, 5)
    STAMP(27);
    XFIN(6); XFIN(7); XFIN(8);
    {
      const u32x2 a = q == 0 ? ov[6] : (q == 1 ? ov[7] : ov[8]);
      *(u32x2*)(op + (6 + (q > 2 ? 2 : q)) * 32) = a;
    }
    asm volatile("fk_code_b:");
    DIAG_END(0);
  }
}

}

extern "C" void kernel_launch(void* const* d_in, const int* in_sizes, int n_in, void* d_out, int out_size,
                              void* d_ws, size_t ws_size, hipStream_t stream) {
  Params P;
  P.x = (const char*)d_in[0]; P.wqkv = (const char*)d_in[1]; P.wo = (const char*)d_in[2];
  P.wg = (const char*)d_in[4]; P.wu = (const char*)d_in[5]; P.wd = (const char*)d_in[6];
  P.gw = (const char*)d_in[7]; P.out = (char*)d_out; P.ws = (char*)d_ws;
  (void)hipFuncSetAttribute((const void*)fused_kernel, hipFuncAttributeMaxDynamicSharedMemorySize, 163840);
  fused_kernel<<<256, 512, 163840, stream>>>(P);
}
```
